```python
import torch

def _softmax(x, axis=-1):
    return torch.softmax(x, dim=axis)
def _gelu(x):
    return torch.nn.functional.gelu(x, approximate="tanh")
def _silu(x):
    return torch.nn.functional.silu(x)
def _sigmoid(x):
    return torch.sigmoid(x)
def _layernorm(x, axis=-1, eps=1e-5):
    m = x.mean(dim=axis, keepdim=True)
    v = x.var(dim=axis, keepdim=True, unbiased=False)
    return (x - m) / torch.sqrt(v + eps)
def _scatter_add(tgt, idx, src, axis=0):
    return tgt.clone().index_add_(axis, idx, src)
def _segment_sum(src, seg, n):
    out = torch.zeros((n,) + src.shape[1:], dtype=src.dtype)
    return out.index_add_(0, seg, src)
def _pad(x, pw):
    flat = tuple(v for pair in reversed(pw) for v in pair)
    return torch.nn.functional.pad(x, flat)

kernel_name = 'sequential_preLN__ssm_conv_s4__dense_activation_squared_relu__prompt'

def setup_inputs():

    _w = (lambda a: (lambda t: t.to(torch.bfloat16) if t.is_floating_point() else t)(torch.as_tensor(a)))
    def _A_setup():
        B, L, D, N = 1, 2048, 512, 128
        A_re = -(lambda _x: torch.exp(torch.as_tensor(_x)))((lambda *s: torch.randn(*s, dtype=torch.bfloat16) * 3.2)(D, N))
        A_im = torch.pi * (lambda *s: torch.randn(*s, dtype=torch.bfloat16) * 0.05)(D, N)
        B_re = (lambda *s: torch.randn(*s, dtype=torch.bfloat16) * 0.1)(D, N)
        B_im = (lambda *s: torch.randn(*s, dtype=torch.bfloat16) * 0.1)(D, N)
        C_re = (lambda *s: torch.randn(*s, dtype=torch.bfloat16) * 0.1)(D, N)
        C_im = (lambda *s: torch.randn(*s, dtype=torch.bfloat16) * 0.1)(D, N)
        log_dt = (lambda *s: torch.randn(*s, dtype=torch.bfloat16) * 0.05)(D) * 0.5 - 4.0
        Dskip = (lambda *s: torch.randn(*s, dtype=torch.bfloat16) * 0.05)(D)
        return {
            "u": _w((lambda *s: torch.randn(*s, dtype=torch.bfloat16) * 0.05)(B, L, D)),
            "A_re": _w(A_re),
            "A_im": _w(A_im),
            "B_re": _w(B_re),
            "B_im": _w(B_im),
            "C_re": _w(C_re),
            "C_im": _w(C_im),
            "log_dt": _w(log_dt),
            "Dskip": _w(Dskip),
        }
    _A_in = _A_setup()

    def _B_setup():
        B, S, D, F = 1, 2048, 512, 1792
        return {
            "x": _w((lambda *s: torch.randn(*s, dtype=torch.bfloat16) * 0.05)(B, S, D)),
            "w1": _w((lambda *s: torch.randn(*s, dtype=torch.bfloat16) * 0.05)(D, F)),
            "b1": _w((lambda *s: torch.randn(*s, dtype=torch.bfloat16) * 0.2)(F)),
            "w2": _w((lambda *s: torch.randn(*s, dtype=torch.bfloat16) * 0.05)(F, D)),
            "b2": _w((lambda *s: torch.randn(*s, dtype=torch.bfloat16) * 1.6)(D)),
        }
    _B_in = _B_setup()

    return {
        "A_u": _A_in["u"],
        "A_A_re": _A_in["A_re"],
        "A_A_im": _A_in["A_im"],
        "A_B_re": _A_in["B_re"],
        "A_B_im": _A_in["B_im"],
        "A_C_re": _A_in["C_re"],
        "A_C_im": _A_in["C_im"],
        "A_log_dt": _A_in["log_dt"],
        "A_Dskip": _A_in["Dskip"],
        "B_x": _B_in["x"],
        "B_w1": _B_in["w1"],
        "B_b1": _B_in["b1"],
        "B_w2": _B_in["w2"],
        "B_b2": _B_in["b2"],
    }

def reference(A_u, A_A_re, A_A_im, A_B_re, A_B_im, A_C_re, A_C_im, A_log_dt, A_Dskip, B_x, B_w1, B_b1, B_w2, B_b2):
    def _A_ref(u, A_re, A_im, B_re, B_im, C_re, C_im, log_dt, Dskip):
        Bsz, L, D = u.shape
        dt = torch.exp(log_dt)[:, None]
        A = A_re + 1j * A_im
        Bc = B_re + 1j * B_im
        Cc = C_re + 1j * C_im
        logdA = dt * A
        dB = dt * Bc
        l = (lambda x, dt: x.to(dt))(torch.arange(L), A_re.dtype)
        P = torch.exp(logdA[:, :, None] * l[None, None, :])
        K = torch.einsum("dn,dnl,dn->dl", Cc, P, dB).real
        L2 = 2 * L
        Kp = _pad(K, ((0, 0), (0, L)))
        up = (lambda x, *d: x.permute(*d))(u, 0, 2, 1)
        up = _pad(up, ((0, 0), (0, 0), (0, L)))
        Kf = (lambda x, axes=(-2,-1): torch.fft.rfft2(x.float(), dim=axes))(Kp[:, None, :], axes=(-2, -1))
        Uf = (lambda x, axes=(-2,-1): torch.fft.rfft2(x.float(), dim=axes))(up[:, :, None, :], axes=(-2, -1))
        Yf = Uf * Kf[None, :, :, :]
        y = (lambda x, s, axes=(-2,-1): torch.fft.irfft2(x, s=s, dim=axes).to(torch.bfloat16))(Yf, (1, L2), axes=(-2, -1))[:, :, 0, :L]
        y = (lambda x, *d: x.permute(*d))(y, 0, 2, 1) + u * Dskip[None, None, :]
        return y

    def _B_ref(x, w1, b1, w2, b2):
        h = torch.einsum("bsd,df->bsf", x, w1) + b1
        zero = torch.zeros_like(h)
        r = torch.where(h > zero, h, zero)
        h = r * r
        out = torch.einsum("bsf,fd->bsd", h, w2) + b2
        return out

    x = A_u
    h = x + _A_ref(_layernorm(x, axis=-1), A_A_re, A_A_im, A_B_re, A_B_im, A_C_re, A_C_im, A_log_dt, A_Dskip)
    h2 = h + _B_ref(_layernorm(h, axis=-1), B_w1, B_b1, B_w2, B_b2)
    return h2
```

```cpp
#include <hip/hip_runtime.h>
#include <hip/hip_bf16.h>
#include <stdint.h>
typedef unsigned short u16;
#define L_ 2048
#define D_ 512
#define N_ 128
#define F_ 1792
__device__ __forceinline__ float bf2f(u16 v){ return __uint_as_float(((unsigned)v)<<16); }
__device__ __forceinline__ u16 f2bf(float f){ unsigned u=__float_as_uint(f); return (u16)((u + 0x7FFFu + ((u>>16)&1u))>>16); }
__device__ __forceinline__ float rbf(float f){ return bf2f(f2bf(f)); }

__global__ void k_ln(const u16* x, u16* u){
  int t = blockIdx.x; int tid = threadIdx.x;
  float v[8]; float s=0;
  for(int i=0;i<8;i++){ v[i]=bf2f(x[t*D_+tid*8+i]); s+=v[i]; }
  for(int o=32;o>0;o>>=1) s+=__shfl_xor(s,o);
  float m = s/D_;
  float q=0; for(int i=0;i<8;i++){ float d=v[i]-m; q+=d*d; }
  for(int o=32;o>0;o>>=1) q+=__shfl_xor(q,o);
  float var = rbf(q/D_); float mb = rbf(m);
  float sd = rbf(sqrtf(rbf(var+1e-5f)));
  for(int i=0;i<8;i++){ u[t*D_+tid*8+i]=f2bf(rbf(v[i]-mb)/sd); }
}
__global__ void k_kgen(const u16* Are,const u16* Aim,const u16* Bre,const u16* Bim,const u16* Cre,const u16* Cim,const u16* logdt,float* K){
  int d = blockIdx.y; int l = blockIdx.x*256+threadIdx.x;
  float dt = rbf(expf(bf2f(logdt[d])));
  float lf = rbf((float)l);
  float acc=0;
  for(int n=0;n<N_;n++){
    float ar=dt*bf2f(Are[d*N_+n]), ai=dt*bf2f(Aim[d*N_+n]);
    float br=dt*bf2f(Bre[d*N_+n]), bi=dt*bf2f(Bim[d*N_+n]);
    float cr=bf2f(Cre[d*N_+n]), ci=bf2f(Cim[d*N_+n]);
    float wr=cr*br-ci*bi, wi=cr*bi+ci*br;
    float e=expf(ar*lf); float sn,cs; sincosf(ai*lf,&sn,&cs);
    acc += e*(wr*cs-wi*sn);
  }
  K[d*L_+l]=acc;
}
__global__ void k_conv(const u16* x,const u16* u,const float* K,const u16* Dskip,u16* h){
  __shared__ float su[L_]; __shared__ float sk[L_];
  int d=blockIdx.x;
  for(int i=threadIdx.x;i<L_;i+=256){ su[i]=bf2f(u[i*D_+d]); sk[i]=K[d*L_+i]; }
  __syncthreads();
  float ds=bf2f(Dskip[d]);
  for(int t=threadIdx.x;t<L_;t+=256){
    float a=0; for(int s=0;s<=t;s++) a+=sk[t-s]*su[s];
    float y=rbf(a); y=rbf(y+rbf(su[t]*ds));
    h[t*D_+d]=f2bf(bf2f(x[t*D_+d])+y);
  }
}
__global__ void k_g1(const u16* a,const u16* w1,const u16* b1,u16* H){
  __shared__ float sa[D_];
  int t=blockIdx.x; for(int i=threadIdx.x;i<D_;i+=256) sa[i]=bf2f(a[t*D_+i]);
  __syncthreads();
  for(int f=threadIdx.x;f<F_;f+=256){ float acc=0; for(int k=0;k<D_;k++) acc+=sa[k]*bf2f(w1[k*F_+f]);
    float v=rbf(rbf(acc)+bf2f(b1[f])); v=v>0?v:0; H[t*F_+f]=f2bf(v*v);} }
__global__ void k_g2(const u16* H,const u16* w2,const u16* b2,const u16* h,u16* out){
  __shared__ float sa[F_];
  int t=blockIdx.x; for(int i=threadIdx.x;i<F_;i+=256) sa[i]=bf2f(H[t*F_+i]);
  __syncthreads();
  for(int d=threadIdx.x;d<D_;d+=256){ float acc=0; for(int k=0;k<F_;k++) acc+=sa[k]*bf2f(w2[k*D_+d]);
    float v=rbf(rbf(acc)+bf2f(b2[d])); out[t*D_+d]=f2bf(bf2f(h[t*D_+d])+v);} }

extern "C" void kernel_launch(void* const* d_in, const int* in_sizes, int n_in, void* d_out, int out_size, void* d_ws, size_t ws_size, hipStream_t stream){
  const u16* x=(const u16*)d_in[0];
  char* ws=(char*)d_ws;
  u16* u=(u16*)ws; float* K=(float*)(ws+(4<<20)); u16* h=(u16*)(ws+(16<<20)); u16* a2=(u16*)(ws+(20<<20)); u16* H=(u16*)(ws+(32<<20));
  k_ln<<<L_,64,0,stream>>>(x,u);
  k_kgen<<<dim3(L_/256,D_),256,0,stream>>>((const u16*)d_in[1],(const u16*)d_in[2],(const u16*)d_in[3],(const u16*)d_in[4],(const u16*)d_in[5],(const u16*)d_in[6],(const u16*)d_in[7],K);
  k_conv<<<D_,256,0,stream>>>(x,u,K,(const u16*)d_in[8],h);
  k_ln<<<L_,64,0,stream>>>(h,a2);
  k_g1<<<L_,256,0,stream>>>(a2,(const u16*)d_in[10],(const u16*)d_in[11],H);
  k_g2<<<L_,256,0,stream>>>(H,(const u16*)d_in[12],(const u16*)d_in[13],h,(u16*)d_out);
}
```

```cpp
#include <hip/hip_runtime.h>
#include <stdint.h>

typedef unsigned short u16;
typedef unsigned int u32;
typedef unsigned long long u64;
typedef u32 u32x2 __attribute__((ext_vector_type(2)));
typedef u32 u32x4 __attribute__((ext_vector_type(4)));
typedef float f32x2 __attribute__((ext_vector_type(2)));
typedef float f32x4 __attribute__((ext_vector_type(4)));
typedef float f32x16 __attribute__((ext_vector_type(16)));
typedef short s16x4 __attribute__((ext_vector_type(4)));
typedef __bf16 bf16x2 __attribute__((ext_vector_type(2)));
typedef __bf16 bf16x8 __attribute__((ext_vector_type(8)));
typedef _Float16 f16x2 __attribute__((ext_vector_type(2)));
typedef _Float16 f16x8 __attribute__((ext_vector_type(8)));
#define LDS3 __attribute__((address_space(3)))
#define DEVI __device__ __forceinline__

#define WS_EP   0u
#define WS_S    4096u
#define WS_X    (1u << 20)
#define WS_HX   (5u << 20)
#define WS_HH   (7u << 20)
#define WS_DBG  (16u << 20)
#define WS_BYTES (20u << 20)
#define POISON  0xFFFFFFFFu
#define SPIN_CAP 200000
#define NB_W2 16

#define L_W1    0
#define L_XT    0
#define L_ST    8448
#define L_SC    8512
#define L_KC    52992
#define KC_STR  2176
#define L_LO    57344
#define L_HI    (L_LO + 33792)
#define L_R     114688
#define L_U     147968
#define L_Y     157184
#define LDS_BYTES 163840

DEVI float bf_lo(u32 v) { return __uint_as_float(v << 16); }
DEVI float bf_hi(u32 v) { return __uint_as_float(v & 0xFFFF0000u); }
DEVI u32 pk_bf(float a, float b) { f32x2 t = {a, b}; return __builtin_bit_cast(u32, __builtin_convertvector(t, bf16x2)); }
DEVI u32 pk_h(f32x2 t) { return __builtin_bit_cast(u32, __builtin_convertvector(t, f16x2)); }
DEVI float rbf(float a) { return bf_lo(pk_bf(a, a)); }
DEVI f32x2 cmul(f32x2 p, f32x2 c, f32x2 crot) {
  f32x2 t = (f32x2){p.x, p.x} * c;
  return __builtin_elementwise_fma((f32x2){p.y, p.y}, crot, t);
}
DEVI f32x2 rot(f32x2 c) { return (f32x2){-c.y, c.x}; }
DEVI f32x2 cexp(float a, float b) {
  float mag = __builtin_amdgcn_exp2f(a * 1.44269504088896341f);
  const float c_hi = 0.15915494309189535f;
  const float c_lo = (float)(0.15915494309189533577 - (double)c_hi);
  float hi = b * c_hi;
  float lo = __builtin_fmaf(b, c_hi, -hi);
  lo = __builtin_fmaf(b, c_lo, lo);
  float fr = (hi - __builtin_rintf(hi)) + lo;
  return (f32x2){mag * __builtin_amdgcn_cosf(fr), mag * __builtin_amdgcn_sinf(fr)};
}
template <int CTRL> DEVI float dppf(float v) {
  return __builtin_bit_cast(float, __builtin_amdgcn_update_dpp(0, __builtin_bit_cast(int, v), CTRL, 0xF, 0xF, true));
}
DEVI float swap16f(float v, bool add) { return v; }
DEVI float wave_sum(float v) {
  v += dppf<0xB1>(v);
  v += dppf<0x4E>(v);
  v += dppf<0x141>(v);
  v += dppf<0x140>(v);
  { auto r = __builtin_amdgcn_permlane16_swap(__float_as_uint(v), __float_as_uint(v), false, false); v = __uint_as_float(r[0]) + __uint_as_float(r[1]); }
  { auto r = __builtin_amdgcn_permlane32_swap(__float_as_uint(v), __float_as_uint(v), false, false); v = __uint_as_float(r[0]) + __uint_as_float(r[1]); }
  return v;
}
DEVI float wave_max(float v) {
  v = fmaxf(v, dppf<0xB1>(v));
  v = fmaxf(v, dppf<0x4E>(v));
  v = fmaxf(v, dppf<0x141>(v));
  v = fmaxf(v, dppf<0x140>(v));
  { auto r = __builtin_amdgcn_permlane16_swap(__float_as_uint(v), __float_as_uint(v), false, false); v = fmaxf(__uint_as_float(r[0]), __uint_as_float(r[1])); }
  { auto r = __builtin_amdgcn_permlane32_swap(__float_as_uint(v), __float_as_uint(v), false, false); v = fmaxf(__uint_as_float(r[0]), __uint_as_float(r[1])); }
  return v;
}
DEVI float rows_sum(float v) {
  { auto r = __builtin_amdgcn_permlane16_swap(__float_as_uint(v), __float_as_uint(v), false, false); v = __uint_as_float(r[0]) + __uint_as_float(r[1]); }
  { auto r = __builtin_amdgcn_permlane32_swap(__float_as_uint(v), __float_as_uint(v), false, false); v = __uint_as_float(r[0]) + __uint_as_float(r[1]); }
  return v;
}
#define BAR() asm volatile("s_waitcnt lgkmcnt(0)\n\ts_barrier" ::: "memory")
#define MEMB() asm volatile("" ::: "memory")
#define VMWAIT0() asm volatile("s_waitcnt vmcnt(0)" ::: "memory")
#define SC1 16

DEVI u32x4 mk_rsrc(const void* p, u32 bytes) {
  u64 a = (u64)p;
  u32x4 r = {(u32)a, (u32)(a >> 32) & 0xFFFFu, bytes, 0x00020000u};
  return r;
}
DEVI void dma16(u32 lds, u32 voff, u32x4 rs, u32 soff) {
  asm volatile("s_mov_b32 m0, %0\n\ts_nop 0\n\tbuffer_load_dwordx4 %1, %2, %3 offen lds" ::"s"(lds), "v"(voff), "s"(rs), "s"(soff) : "memory");
}
template <typename T> DEVI T lds_ld(u32 a) { return *(const LDS3 T*)a; }
template <typename T> DEVI void lds_st(u32 a, T v) { *(LDS3 T*)a = v; }
DEVI s16x4 lds_tr(u32 a) { return __builtin_amdgcn_ds_read_tr16_b64_v4i16((LDS3 s16x4*)a); }
DEVI bf16x8 cat8(s16x4 a, s16x4 b) {
  u32x2 x = __builtin_bit_cast(u32x2, a), y = __builtin_bit_cast(u32x2, b);
  u32x4 r = {x.x, x.y, y.x, y.y};
  return __builtin_bit_cast(bf16x8, r);
}
DEVI f32x4 mfma16(bf16x8 a, bf16x8 b, f32x4 c) { return __builtin_amdgcn_mfma_f32_16x16x32_bf16(a, b, c, 0, 0, 0); }
DEVI f32x16 mfma32(bf16x8 a, bf16x8 b, f32x16 c) { return __builtin_amdgcn_mfma_f32_32x32x16_bf16(a, b, c, 0, 0, 0); }
DEVI u32 max3u(u32 a, u32 b, u32 c) { return max(max(a, b), c); }
DEVI u32 max4(u32 m, u32x4 v) { return max3u(max3u(m, v.x, v.y), v.z, v.w); }

#define STAMP(i)
#define STAMPV(i, val)

template <int SET> DEVI void conv_wave(u32 sb, int ch, int lane) {
  constexpr int NF = SET == 0 ? 8 : 6;
  const int b = lane & 15, g = lane >> 4;
  u32 baseA = sb + L_R + ch * 16640 + (b & 3) * 4160 + 2 * (2028 - 4 * (b >> 2) + 8 * g) - 512 * 7;
  u32 baseB = sb + L_U + ch * 4608 + 2 * (256 + 16 * b - 16 + 8 * g);
  f32x4 acc[8];
#pragma unroll
  for (int t = 0; t < 8; t++) acc[t] = (f32x4){0.f, 0.f, 0.f, 0.f};
  bf16x8 A[2][NF], B[2][NF];
  auto ldf = [&](int buf, int it) {
#pragma unroll
    for (int q = 0; q < NF; q++) {
      u32 a = baseA - 64 * it + 512 * (7 - q);
      u32x2 lo = lds_ld<u32x2>(a), hi = lds_ld<u32x2>(a + 8);
      u32x4 r = {lo.x, lo.y, hi.x, hi.y};
      A[buf][q] = __builtin_bit_cast(bf16x8, r);
      B[buf][q] = __builtin_bit_cast(bf16x8, lds_ld<u32x4>(baseB - 64 * it + 512 * q));
    }
  };
  auto mm = [&](int buf) {
#pragma unroll
    for (int t = 0; t < 8; t++) {
      const bool mine = SET == 0 ? (t == 7 || t == 6 || t == 2) : (t == 5 || t == 4 || t == 3 || t == 1 || t == 0);
      if (mine) {
#pragma unroll
        for (int q = 0; q <= t; q++) acc[t] = mfma16(A[buf][q], B[buf][t - q], acc[t]);
      }
    }
  };
  ldf(0, 0);
#pragma unroll
  for (int it = 0; it < 8; it++) {
    if (it + 1 < 8) ldf((it + 1) & 1, it + 1);
    mm(it & 1);
  }
#pragma unroll
  for (int t = 0; t < 8; t++) {
    const bool mine = SET == 0 ? (t == 7 || t == 6 || t == 2) : (t == 5 || t == 4 || t == 3 || t == 1 || t == 0);
    if (mine) {
      u32x2 v = {pk_bf(acc[t].x, acc[t].y), pk_bf(acc[t].z, acc[t].w)};
      u32 a = (t >= 6 ? sb + L_U + ch * 4608 + 512 : sb + L_Y + ch * 3072) + 512 * t + 32 * b + 8 * g;
      lds_st<u32x2>(a, v);
    }
  }
}

DEVI constexpr u32 w2slot(int kh, int slot) { return slot < 12 ? (u32)(L_R + kh * 24576 + slot * 2048) : (u32)(kh * 57344 + (slot - 12) * 2048); }
DEVI constexpr int ord2(int e) { return e < 32 ? (e >> 2) * 7 + (e & 3) : ((e - 32) / 3) * 7 + 4 + (e - 32) % 3; }

__global__ void __launch_bounds__(512, 2)
s4ffn_fused(const u16* __restrict__ x, const u16* __restrict__ Are, const u16* __restrict__ Aim, const u16* __restrict__ Bre,
            const u16* __restrict__ Bim, const u16* __restrict__ Cre, const u16* __restrict__ Cim, const u16* __restrict__ logdt,
            const u16* __restrict__ Dsk, const u16* __restrict__ w1, const u16* __restrict__ b1, const u16* __restrict__ w2,
            const u16* __restrict__ b2, u16* __restrict__ out, char* __restrict__ ws) {
  __shared__ __attribute__((aligned(16))) char smem[LDS_BYTES];
  const u32 sb = (u32)(uintptr_t)(LDS3 char*)smem;
  const int tid = threadIdx.x, lane = tid & 63;
  const int w = __builtin_amdgcn_readfirstlane(tid >> 6);
  const int bid = blockIdx.x, xcd = bid & 7, idx = bid >> 3, lin = xcd * 32 + idx;
  const __amdgpu_buffer_rsrc_t rw = __builtin_amdgcn_make_buffer_rsrc(ws, 0, WS_BYTES, 0x00020000);
  STAMP(0);
  __builtin_amdgcn_raw_buffer_store_b128((u32x4){POISON, POISON, POISON, POISON}, rw, WS_HX + (lin * 8 + w) * 1024 + lane * 16, 0, SC1);
  const u32 ep = __builtin_amdgcn_raw_buffer_load_b32(rw, WS_EP, 0, SC1);
  const u32x4 row = *(const u32x4*)(x + (lin * 8 + w) * 512 + lane * 8);
  const int pi = lin * 256 + (tid & 255);
  const int chw = (w >> 1) & 1;
  const u32 are = Are[pi], aim = Aim[pi];
  u32 bre = 0, bim = 0, cre = 0, cim = 0, b2r = 0, b2i = 0, c2r = 0, c2i = 0;
  if (w >= 4) {
    bre = Bre[pi]; bim = Bim[pi]; cre = Cre[pi]; cim = Cim[pi];
    const int p2 = pi ^ 64;
    b2r = Bre[p2]; b2i = Bim[p2]; c2r = Cre[p2]; c2i = Cim[p2];
  }
  u32 pfv;
  { u64 pc; asm volatile("s_getpc_b64 %0" : "=s"(pc)); pfv = __builtin_nontemporal_load((const u32*)((pc & ~127ull) + (u64)(tid & 255) * 128)); }
  const float ldt = bf_lo(logdt[lin * 2 + chw]);
  if (tid < 64) lds_st<u32x4>(sb + L_U + (tid >> 5) * 4608 + (tid & 31) * 16, (u32x4){0, 0, 0, 0});
  else if (tid < 96) lds_st<u32x4>(sb + L_R + ((tid - 64) >> 2) * 4160 + 4096 + (tid & 3) * 16, (u32x4){0, 0, 0, 0});
  else if (tid < 98) lds_st<u32x4>(sb + L_KC + (tid - 96) * KC_STR + 2048, (u32x4){0, 0, 0, 0});
  {
    lds_st<u32x4>(sb + L_XT + w * 1040 + lane * 16, row);
    float v[8] = {bf_lo(row.x), bf_hi(row.x), bf_lo(row.y), bf_hi(row.y), bf_lo(row.z), bf_hi(row.z), bf_lo(row.w), bf_hi(row.w)};
    float s = ((v[0] + v[1]) + (v[2] + v[3])) + ((v[4] + v[5]) + (v[6] + v[7]));
    const float m = wave_sum(s) * (1.f / 512.f);
    float q = 0.f;
#pragma unroll
    for (int i = 0; i < 8; i++) { float d = v[i] - m; q = __builtin_fmaf(d, d, q); }
    const float var = rbf(wave_sum(q) * (1.f / 512.f));
    const float sd = __builtin_sqrtf(rbf(var + 1e-5f));
    if (lane == 0) lds_st<u32>(sb + L_ST + w * 4, pk_bf(m, sd));
  }
  const u32 tag = ep * 1664525u + 1013904223u;
  STAMP(1);
  BAR();
  {
#pragma unroll
    for (int i = 0; i < 2; i++) {
      const int p = (tid >> 2) + 128 * i, tq = tid & 3;
      u32 a0 = lds_ld<u32>(sb + L_XT + (2 * tq) * 1040 + p * 4), a1 = lds_ld<u32>(sb + L_XT + (2 * tq + 1) * 1040 + p * 4);
      __builtin_amdgcn_raw_buffer_store_b128((u32x4){a0, tag, a1, tag}, rw, WS_X + p * 16384 + (lin * 8 + 2 * tq) * 8, 0, SC1);
    }
    if (tid < 4) {
      u32x2 s2 = lds_ld<u32x2>(sb + L_ST + tid * 8);
      __builtin_amdgcn_raw_buffer_store_b128((u32x4){s2.x, tag, s2.y, tag}, rw, WS_S + (lin * 8 + 2 * tid) * 8, 0, SC1);
    }
  }
  STAMP(2);
  const float dt = rbf(__expf(ldt));
  const float zr = dt * bf_lo(are), zi = dt * bf_lo(aim);
  const int g1_panel = (xcd >> 1) * 4 + (idx >> 3), g1_cr = (xcd & 1) * 8 + (idx & 7);
  const int g2_tr = xcd * 4 + (idx >> 3), g2_ct = idx & 7;

  if (w < 4) {
    {
      const f32x2 c = cexp(zr * 8.f, -zi * 8.f);
      const f32x2 c2 = cmul(c, c, rot(c)), c2r = rot(c2);
      f32x2 v0 = {1.f, 0.f}, v1 = c;
      const u32 a = sb + L_LO + chw * 16896 + (tid & 127) * 4;
#pragma unroll
      for (int p = 0; p < 32; p += 2) {
        lds_st<u32>(a + p * 528, pk_h(v0));
        lds_st<u32>(a + (p + 1) * 528, pk_h(v1));
        if (p + 2 < 32) { v0 = cmul(v0, c2, c2r); v1 = cmul(v1, c2, c2r); }
      }
    }
    STAMP(3);
    BAR();
    {
      const int r = lane & 15, g = lane >> 4, ph = w & 1;
      const u32 aH0 = sb + L_HI + chw * 11088 + r * 528 + g * 16;
      const u32 aH1 = sb + L_HI + chw * 11088 + min(16 + r, 20) * 528 + g * 16;
      const u32 aL = sb + L_LO + chw * 16896 + (ph * 16 + r) * 528 + g * 16;
      f32x4 k0 = {0.f, 0.f, 0.f, 0.f}, k1 = {0.f, 0.f, 0.f, 0.f};
#pragma unroll
      for (int s = 0; s < 8; s++) {
        f16x8 h0 = lds_ld<f16x8>(aH0 + s * 64), h1 = lds_ld<f16x8>(aH1 + s * 64), l = lds_ld<f16x8>(aL + s * 64);
        k0 = __builtin_amdgcn_mfma_f32_16x16x32_f16(h0, l, k0, 0, 0, 0);
        k1 = __builtin_amdgcn_mfma_f32_16x16x32_f16(h1, l, k1, 0, 0, 0);
      }
      const float sc = lds_ld<float>(sb + L_SC + chw * 4);
      const int p = ph * 16 + r;
      const u32 kc = sb + L_KC + chw * KC_STR;
      const int i0 = g < 2 ? 8 * p + 4 * g : (g == 2 ? 256 + 4 * p : 384 + 2 * p);
      const int i1 = g < 3 ? i0 + 2 : 448 + 2 * p;
      lds_st<u32>(kc + 2 * i0, pk_bf(k0.x * sc, k0.y * sc));
      lds_st<u32>(kc + 2 * i1, pk_bf(k0.z * sc, k0.w * sc));
      const u32 e0 = pk_bf(k1.x * sc, k1.y * sc), e1 = pk_bf(k1.z * sc, k1.w * sc);
      const u32 a2 = kc + 2 * (512 + 128 * g + p);
      lds_st<u16>(a2, (u16)e0); lds_st<u16>(a2 + 64, (u16)(e0 >> 16)); lds_st<u16>(a2 + 128, (u16)e1); lds_st<u16>(a2 + 192, (u16)(e1 >> 16));
    }
    STAMP(4);
    BAR();
    {
      u32 ga[11];
#pragma unroll
      for (int i = 0; i < 11; i++) {
        const int l = 2047 - 8 * tid - i;
        int id = (int)(pk_bf((float)l, 0.f) & 0xFFFFu) - 17024;
        id = l < 128 ? l : id;
        id = l < 0 ? 1024 : id;
        ga[i] = sb + L_KC + 2 * id;
      }
#pragma unroll
      for (int ch = 0; ch < 2; ch++) {
        u32 wv[11];
#pragma unroll
        for (int i = 0; i < 11; i++) wv[i] = lds_ld<u16>(ga[i] + ch * KC_STR);
        u32 d[10];
#pragma unroll
        for (int i = 0; i < 10; i++) d[i] = wv[i] | (wv[i + 1] << 16);
        const u32 a = sb + L_R + ch * 16640 + tid * 16;
#pragma unroll
        for (int c = 0; c < 4; c++) lds_st<u32x4>(a + c * 4160, (u32x4){d[3 - c], d[5 - c], d[7 - c], d[9 - c]});
      }
    }
    STAMP(5);
    BAR();
    STAMP(6);
    if (bid == 0 && tid == 0) __builtin_amdgcn_raw_buffer_store_b32(tag, rw, WS_EP, 0, SC1);
    const int tb1 = g1_panel * 4 + w;
    const u32 hh_out = WS_HH + (tb1 * 112 + g1_cr * 7) * 1024 + lane * 16;
    STAMP(7);
    if (w & 1) conv_wave<1>(sb, w >> 1, lane); else conv_wave<0>(sb, w >> 1, lane);
    STAMP(8);
    BAR();
    BAR();
    STAMP(9);
    const int r = lane & 15, g = lane >> 4;
    const u32 wb0 = sb + L_W1 + (lane >> 2) * 224 + (lane & 3) * 8, wb1 = wb0 + 57344;
    auto wfrag = [&](int s, int c) {
      const u32 a = (s < 8 ? wb0 : wb1) + ((s & 7) * 32) * 224 + c * 32;
      return cat8(lds_tr(a), lds_tr(a + 16 * 224));
    };
    s16x4 aux[7];
    {
      u32 b1v[7];
#pragma unroll
      for (int c = 0; c < 7; c++) b1v[c] = b1[g1_cr * 112 + c * 16 + r];
      const u32x4 o4 = {0x3F803F80u, 0x3F803F80u, 0x3F803F80u, 0x3F803F80u};
      const bf16x8 ones = __builtin_bit_cast(bf16x8, o4);
      f32x4 cs[7];
#pragma unroll
      for (int c = 0; c < 7; c++) cs[c] = (f32x4){0.f, 0.f, 0.f, 0.f};
#pragma unroll
      for (int s = 0; s < 16; s++)
#pragma unroll
        for (int c = 0; c < 7; c++) cs[c] = mfma16(ones, wfrag(s, c), cs[c]);
#pragma unroll
      for (int c = 0; c < 7; c++) {
        const float v = cs[c].x, h0 = rbf(v), h1 = rbf(v - h0), h2 = v - h0 - h1;
        u32x2 t = {pk_bf(h0, h1), (pk_bf(h2, 0.f) & 0xFFFFu) | (b1v[c] << 16)};
        if (g != 0) t = (u32x2){0, 0};
        aux[c] = __builtin_bit_cast(s16x4, t);
      }
    }
    STAMP(10);
    const u32 hx_in = WS_HX + tb1 * 32768 + lane * 16;
    u32x4 raw[32];
    bf16x8 F[16][2];
    f32x4 acc[7][2];
#pragma unroll
    for (int c = 0; c < 7; c++) { acc[c][0] = (f32x4){0.f, 0.f, 0.f, 0.f}; acc[c][1] = (f32x4){0.f, 0.f, 0.f, 0.f}; }
    float sm[2] = {0.f, 0.f}, sq[2] = {0.f, 0.f};
    const bf16x2 one2 = __builtin_bit_cast(bf16x2, 0x3F803F80u);
    constexpr int PF1 = 12;
    int retries = 0;
#pragma unroll
    for (int i = 0; i < PF1; i++) raw[i] = __builtin_amdgcn_raw_buffer_load_b128(rw, hx_in, i * 1024, SC1);
    const bool lowr = r < 8;
#pragma unroll
    for (int gi = 0; gi <= 8; gi++) {
      if (gi < 8) {
        u32 mx = max4(max4(max4(max4(0u, raw[4 * gi]), raw[4 * gi + 1]), raw[4 * gi + 2]), raw[4 * gi + 3]);
        if (__builtin_expect(__any(mx == POISON), 0)) {
          int sp = 0;
          do {
            __builtin_amdgcn_s_sleep(2);
#pragma unroll
            for (int i = 4 * gi; i < (4 * gi + PF1 < 32 ? 4 * gi + PF1 : 32); i++) raw[i] = __builtin_amdgcn_raw_buffer_load_b128(rw, hx_in, i * 1024, SC1);
            mx = max4(max4(max4(max4(0u, raw[4 * gi]), raw[4 * gi + 1]), raw[4 * gi + 2]), raw[4 * gi + 3]);
            MEMB();
            retries++;
          } while (__any(mx == POISON) && ++sp < SPIN_CAP);
        }
#pragma unroll
        for (int i = 4 * gi + PF1; i < 4 * gi + PF1 + 4; i++)
          if (i < 32) raw[i] = __builtin_amdgcn_raw_buffer_load_b128(rw, hx_in, i * 1024, SC1);
        if (4 * gi + PF1 + 4 == 32) asm volatile("s_barrier" ::: "memory");
#pragma unroll
        for (int s = 2 * gi; s < 2 * gi + 2; s++) {
#pragma unroll
          for (int t = 0; t < 2; t++) {
            u32 f[4];
#pragma unroll
            for (int i = 0; i < 2; i++) {
              const u32 dl = raw[2 * s + i][t], dh = raw[2 * s + i][2 + t];
              const u32 pl = __builtin_amdgcn_update_dpp(0u, dl, 0x128, 0xF, 0xF, true), ph = __builtin_amdgcn_update_dpp(0u, dh, 0x128, 0xF, 0xF, true);
              f[2 * i] = lowr ? dl : ph;
              f[2 * i + 1] = lowr ? pl : dh;
            }
#pragma unroll
            for (int i = 0; i < 4; i++) {
              const bf16x2 e = __builtin_bit_cast(bf16x2, f[i]);
              sm[t] = __builtin_amdgcn_fdot2_f32_bf16(e, one2, sm[t], false);
              sq[t] = __builtin_amdgcn_fdot2_f32_bf16(e, e, sq[t], false);
            }
            F[s][t] = __builtin_bit_cast(bf16x8, (u32x4){f[0], f[1], f[2], f[3]});
          }
        }
      }
      if (gi > 0) {
#pragma unroll
        for (int s = 2 * gi - 2; s < 2 * gi; s++)
#pragma unroll
          for (int c = 0; c < 4; c++) {
            const bf16x8 wf = wfrag(s, c);
            acc[c][0] = mfma16(wf, F[s][0], acc[c][0]);
            acc[c][1] = mfma16(wf, F[s][1], acc[c][1]);
          }
      }
    }
    STAMP(11);
    STAMPV(30, retries);
    float rs2[2];
    s16x4 tokaux[2];
#pragma unroll
    for (int t = 0; t < 2; t++) {
      const float m = rows_sum(sm[t]) * (1.f / 512.f);
      float var = rows_sum(sq[t]) * (1.f / 512.f) - m * m;
      var = rbf(fmaxf(var, 0.f));
      const float sd = rbf(__builtin_sqrtf(rbf(var + 1e-5f)));
      const float ri = __builtin_amdgcn_rcpf(sd);
      rs2[t] = ri;
      u32x2 v = {pk_bf(-m, -m), pk_bf(-m, sd)};
      tokaux[t] = __builtin_bit_cast(s16x4, v);
    }
    auto epi1 = [&](int c) {
      u32 pk[2][2];
#pragma unroll
      for (int t = 0; t < 2; t++) {
        f32x4 a = __builtin_amdgcn_mfma_f32_16x16x16bf16_1k(aux[c], tokaux[t], acc[c][t], 0, 0, 0);
        float e[4];
#pragma unroll
        for (int i = 0; i < 4; i++) { const float v = fmaxf(a[i], 0.f) * rs2[t]; e[i] = v * v; }
        pk[t][0] = pk_bf(e[0], e[1]); pk[t][1] = pk_bf(e[2], e[3]);
      }
      auto s0 = __builtin_amdgcn_permlane16_swap(pk[0][0], pk[1][0], false, false);
      auto s1 = __builtin_amdgcn_permlane16_swap(pk[0][1], pk[1][1], false, false);
      __builtin_amdgcn_raw_buffer_store_b128((u32x4){s0[0], s1[0], s0[1], s1[1]}, rw, hh_out + c * 1024, 0, SC1);
    };
#pragma unroll
    for (int s = 0; s < 16; s++) {
#pragma unroll
      for (int c = 4; c < 7; c++) {
        const bf16x8 wf = wfrag(s, c);
        acc[c][0] = mfma16(wf, F[s][0], acc[c][0]);
        acc[c][1] = mfma16(wf, F[s][1], acc[c][1]);
      }
      if (s < 8 && (s & 1)) epi1(s >> 1);
    }
    STAMP(12);
    BAR();
    const int kh = w >> 1, tb2 = g2_tr * 2 + (w & 1);
    const u32 hh_in = WS_HH + (tb2 * 112 + kh * 56) * 1024 + lane * 16;
    constexpr int PF2 = 12;
    u32x4 hf[56];
#pragma unroll
    for (int e = 0; e < PF2; e++) hf[e] = __builtin_amdgcn_raw_buffer_load_b128(rw, hh_in, ord2(e) * 1024, SC1);
    epi1(4); epi1(5); epi1(6);
    STAMP(13);
    f32x16 o0, o1;
#pragma unroll
    for (int i = 0; i < 16; i++) { o0[i] = 0.f; o1[i] = 0.f; }
    const int q4 = (lane >> 2) & 3;
    const u32 tro = ((lane >> 5) * 8 + q4) * 128 + ((lane >> 4) & 1) * 32 + (lane & 3) * 8;
    const u32 trA = sb + (tro + ((q4 >> 1) & 1) * 64), trB = sb + (tro + (((q4 >> 1) & 1) ^ 1) * 64);
    const u32 offA = kh * 24576, offB = kh * 57344;
    int retries2 = 0;
#pragma unroll
    for (int e0 = 0; e0 < 56;) {
      const int n = e0 < 32 ? 4 : 3;
      if (e0 == 12 || e0 == 24 || e0 == 38) BAR();
      u32 mx = 0;
#pragma unroll
      for (int e = e0; e < e0 + n; e++) mx = max4(mx, hf[e]);
      if (__builtin_expect(__any(mx == POISON), 0)) {
        int sp = 0;
        do {
          __builtin_amdgcn_s_sleep(2);
#pragma unroll
          for (int e = e0; e < (e0 + PF2 < 56 ? e0 + PF2 : 56); e++) hf[e] = __builtin_amdgcn_raw_buffer_load_b128(rw, hh_in, ord2(e) * 1024, SC1);
          mx = 0;
#pragma unroll
          for (int e = e0; e < e0 + n; e++) mx = max4(mx, hf[e]);
          MEMB();
          retries2++;
        } while (__any(mx == POISON) && ++sp < SPIN_CAP);
      }
#pragma unroll
      for (int e = e0; e < e0 + n; e++) {
        if (e + PF2 < 56) hf[e + PF2] = __builtin_amdgcn_raw_buffer_load_b128(rw, hh_in, ord2(e + PF2) * 1024, SC1);
        const int slot = e < 38 ? e : e - 38;
        const u32 so = (slot < 12 ? offA : offB) + w2slot(0, slot);
        const bf16x8 wa = cat8(lds_tr(trA + so), lds_tr(trA + so + 512));
        const bf16x8 wb = cat8(lds_tr(trB + so), lds_tr(trB + so + 512));
        const bf16x8 hb = __builtin_bit_cast(bf16x8, hf[e]);
        o0 = mfma32(wa, hb, o0);
        o1 = mfma32(wb, hb, o1);
      }
      e0 += n;
    }
    STAMP(14);
    STAMPV(31, retries2);
    {
      const int sl = lane & 31, rr = sl & 15;
      const int tok = (w & 1) * 32 + 4 * (rr & 7) + 2 * (rr >> 3) + (sl >> 4);
      const u32 a = sb + kh * 57344 + 16384 + tok * 272 + (lane >> 5) * 16;
#pragma unroll
      for (int i = 0; i < 4; i++) {
        lds_st<f32x4>(a + i * 32, (f32x4){o0[4 * i], o0[4 * i + 1], o0[4 * i + 2], o0[4 * i + 3]});
        lds_st<f32x4>(a + 128 + i * 32, (f32x4){o1[4 * i], o1[4 * i + 1], o1[4 * i + 2], o1[4 * i + 3]});
      }
    }
  } else {
    {
      const float br = bf_lo(bre), bi = bf_lo(bim), cr = bf_lo(cre), ci = bf_lo(cim);
      float bound = fmaxf((fabsf(br) + fabsf(bi)) * (fabsf(cr) + fabsf(ci)),
                          (fabsf(bf_lo(b2r)) + fabsf(bf_lo(b2i))) * (fabsf(bf_lo(c2r)) + fabsf(bf_lo(c2i))));
      bound = wave_max(bound);
      const int ex = __builtin_amdgcn_frexp_expf(bound);
      const f32x2 cc = {__builtin_ldexpf(cr, -ex), __builtin_ldexpf(ci, -ex)};
      const f32x2 W = cmul((f32x2){br, bi}, cc, rot(cc));
      if ((tid & 127) == 0) lds_st<float>(sb + L_SC + chw * 4, __builtin_ldexpf(dt, ex));
      const f32x2 e1 = cexp(zr, zi), e256 = cexp(zr * 256.f, zi * 256.f);
      const f32x2 e1r = rot(e1), e256r = rot(e256);
      const f32x2 e2 = cmul(e1, e1, e1r), e2r = rot(e2), e4 = cmul(e2, e2, e2r), e4r = rot(e4);
      const u32 a = sb + L_HI + chw * 11088 + (tid & 127) * 4;
      f32x2 v = W, G = W;
#pragma unroll
      for (int i = 0; i < 8; i++) {
        lds_st<u32>(a + i * 528, pk_h(v));
        if (i < 7) v = cmul(v, e1, e1r);
        G = cmul(G, e256, e256r);
        if (i == 0) {
          f32x2 t = G;
#pragma unroll
          for (int j = 0; j < 4; j++) { lds_st<u32>(a + (8 + j) * 528, pk_h(t)); if (j < 3) t = cmul(t, e2, e2r); }
        } else if (i < 3) {
          lds_st<u32>(a + (10 + 2 * i) * 528, pk_h(G));
          lds_st<u32>(a + (11 + 2 * i) * 528, pk_h(cmul(G, e4, e4r)));
        } else {
          lds_st<u32>(a + (13 + i) * 528, pk_h(G));
        }
      }
    }
    STAMP(3);
    BAR();
    STAMP(4);
    BAR();
    const int j = w - 4;
    u32 xv[8], uv[8];
    {
      u32x4 gx[4], gs[4];
      const u32 o = (512 * j + 4 * lane) * 8;
      int sp = 0;
      for (;;) {
#pragma unroll
        for (int i = 0; i < 4; i++) gx[i] = __builtin_amdgcn_raw_buffer_load_b128(rw, WS_X + lin * 16384 + o + (i >> 1) * 2048 + (i & 1) * 16, 0, SC1);
#pragma unroll
        for (int i = 0; i < 4; i++) gs[i] = __builtin_amdgcn_raw_buffer_load_b128(rw, WS_S + o + (i >> 1) * 2048 + (i & 1) * 16, 0, SC1);
        u32 bad = 0;
#pragma unroll
        for (int i = 0; i < 4; i++) bad |= (gx[i].y ^ tag) | (gx[i].w ^ tag) | (gs[i].y ^ tag) | (gs[i].w ^ tag);
        if (__all(bad == 0) || ++sp >= SPIN_CAP) break;
        __builtin_amdgcn_s_sleep(8);
        MEMB();
      }
      STAMPV(30, sp);
      STAMP(5);
#pragma unroll
      for (int i = 0; i < 8; i++) {
        const u32 xx = gx[i >> 1][(i & 1) * 2], st = gs[i >> 1][(i & 1) * 2];
        const float m = bf_lo(st), ri = __builtin_amdgcn_rcpf(bf_hi(st));
        const u32 d = pk_bf(bf_lo(xx) - m, bf_hi(xx) - m);
        xv[i] = xx;
        uv[i] = pk_bf(bf_lo(d) * ri, bf_hi(d) * ri);
      }
#pragma unroll
      for (int i = 0; i < 2; i++) {
        const u32 a = sb + L_U + 512 + (512 * j + 256 * i + 4 * lane) * 2;
        lds_st<u32x2>(a, (u32x2){__builtin_amdgcn_perm(uv[4 * i + 1], uv[4 * i], 0x05040100u), __builtin_amdgcn_perm(uv[4 * i + 3], uv[4 * i + 2], 0x05040100u)});
        lds_st<u32x2>(a + 4608, (u32x2){__builtin_amdgcn_perm(uv[4 * i + 1], uv[4 * i], 0x07060302u), __builtin_amdgcn_perm(uv[4 * i + 3], uv[4 * i + 2], 0x07060302u)});
      }
    }
    BAR();
    STAMP(6);
    {
      const u32 hp = WS_HH + ((g1_panel * 4 + j) * 112 + g1_cr * 7) * 1024 + lane * 16;
#pragma unroll
      for (int c = 0; c < 7; c++) __builtin_amdgcn_raw_buffer_store_b128((u32x4){POISON, POISON, POISON, POISON}, rw, hp + c * 1024, 0, SC1);
      const u32x4 r1 = mk_rsrc(w1, 512 * 1792 * 2);
      u32 off[7];
#pragma unroll
      for (int p = 0; p < 7; p++) { const int jj = p * 64 + lane; off[p] = g1_cr * 224 + (j * 128 + jj / 14) * 3584 + (jj % 14) * 16; }
#pragma unroll
      for (int i = 0; i < 28; i++) dma16(sb + L_W1 + (j * 28 + i) * 1024, off[i % 7], r1, (i / 7) * 32 * 3584);
      asm volatile("s_waitcnt vmcnt(28)" ::: "memory");
    }
    const __amdgpu_buffer_rsrc_t r2 = __builtin_amdgcn_make_buffer_rsrc((void*)w2, 0, 1792 * 512 * 2, 0x00020000);
    const int kh = j >> 1, hf = j & 1;
    const u32 voff = (kh * 896 + hf * 8 + (lane >> 3)) * 1024 + g2_ct * 128 + (((lane & 7) ^ (((lane >> 4) & 1) << 2)) * 16);
    const u32 dA = sb + kh * 24576 + hf * 1024 + lane * 16, dB = sb + kh * 57344 + hf * 1024 + lane * 16;
    u32x4 wr[56];
#pragma unroll
    for (int e = 0; e < NB_W2; e++) wr[e] = __builtin_amdgcn_raw_buffer_load_b128(r2, voff, ord2(e) * 16384, 0);
    STAMP(7);
    const u32 dsk = *(const u32*)(Dsk + lin * 2);
#pragma unroll
    for (int i = 0; i < 8; i++) uv[i] = pk_bf(bf_lo(uv[i]) * bf_lo(dsk), bf_hi(uv[i]) * bf_hi(dsk));
    BAR();
    STAMP(9);
    {
      const u32 yb = j == 3 ? sb + L_U + 512 : sb + L_Y, ys = j == 3 ? 4608 : 3072;
#pragma unroll
      for (int i = 0; i < 2; i++) {
        const int t = 512 * j + 256 * i + 4 * lane;
        const u32x2 y0 = lds_ld<u32x2>(yb + 2 * t), y1 = lds_ld<u32x2>(yb + ys + 2 * t);
        u32 hv[4];
#pragma unroll
        for (int k = 0; k < 4; k++) {
          const float a0 = (k & 1) ? bf_hi(y0[k >> 1]) : bf_lo(y0[k >> 1]), a1 = (k & 1) ? bf_hi(y1[k >> 1]) : bf_lo(y1[k >> 1]);
          const u32 s = pk_bf(a0 + bf_lo(uv[4 * i + k]), a1 + bf_hi(uv[4 * i + k]));
          hv[k] = pk_bf(bf_lo(xv[4 * i + k]) + bf_lo(s), bf_hi(xv[4 * i + k]) + bf_hi(s));
        }
        __builtin_amdgcn_raw_buffer_store_b128((u32x4){hv[0], hv[1], hv[2], hv[3]}, rw, WS_HX + (t >> 5) * 32768 + lin * 128 + (t & 31) * 4, 0, SC1);
      }
    }
    STAMP(10);
    asm volatile("" ::"v"(wr[0]) : "memory");
    BAR();
#pragma unroll
    for (int e = 0; e < 12; e++) lds_st<u32x4>(dA + w2slot(0, e), wr[e]);
    BAR();
    STAMP(11);
#pragma unroll
    for (int e = NB_W2; e < 56; e++) wr[e] = __builtin_amdgcn_raw_buffer_load_b128(r2, voff, ord2(e) * 16384, 0);
    STAMP(12);
    BAR();
#pragma unroll
    for (int e = 12; e < 24; e++) lds_st<u32x4>(dB + w2slot(0, e), wr[e]);
    BAR();
#pragma unroll
    for (int e = 24; e < 38; e++) lds_st<u32x4>(dB + w2slot(0, e), wr[e]);
    BAR();
#pragma unroll
    for (int e = 38; e < 56; e++) lds_st<u32x4>((e - 38 < 12 ? dA : dB) + w2slot(0, e - 38), wr[e]);
    BAR();
  }
  {
    const int tok = tid >> 3, oct = tid & 7;
    const int t = g2_tr * 64 + tok;
    const u32 ha = WS_HX + (t >> 5) * 32768 + (g2_ct * 32 + oct * 4) * 128 + (t & 31) * 4;
    u32 hr[4];
    int sp = 0;
    for (;;) {
#pragma unroll
      for (int i = 0; i < 4; i++) hr[i] = __builtin_amdgcn_raw_buffer_load_b32(rw, ha + i * 128, 0, SC1);
      if (__all(max3u(max3u(hr[0], hr[1], hr[2]), hr[3], 0u) != POISON) || ++sp >= SPIN_CAP) break;
      MEMB();
    }
    const u32x4 bb = *(const u32x4*)(b2 + g2_ct * 64 + oct * 8);
    BAR();
    const u32 a = sb + 16384 + tok * 272 + oct * 32;
    const f32x4 p0 = lds_ld<f32x4>(a), p1 = lds_ld<f32x4>(a + 16), p2 = lds_ld<f32x4>(a + 57344), p3 = lds_ld<f32x4>(a + 57344 + 16);
    const f32x4 s0 = p0 + p2, s1 = p1 + p3;
    u32 o[4];
#pragma unroll
    for (int i = 0; i < 4; i++) {
      const float v0 = i < 2 ? s0[2 * i] : s1[2 * i - 4], v1 = i < 2 ? s0[2 * i + 1] : s1[2 * i - 3];
      const u32 r0 = pk_bf(v0, v1);
      const u32 r1 = pk_bf(bf_lo(r0) + bf_lo(bb[i]), bf_hi(r0) + bf_hi(bb[i]));
      o[i] = pk_bf(bf_lo(hr[i]) + bf_lo(r1), bf_hi(hr[i]) + bf_hi(r1));
    }
    *(u32x4*)(out + t * 512 + g2_ct * 64 + oct * 8) = (u32x4){o[0], o[1], o[2], o[3]};
  }
  asm volatile("" ::"v"(pfv));
  STAMP(15);
}

extern "C" void kernel_launch(void* const* d_in, const int* in_sizes, int n_in, void* d_out, int out_size, void* d_ws, size_t ws_size,
                              hipStream_t stream) {
  s4ffn_fused<<<256, 512, 0, stream>>>((const u16*)d_in[0], (const u16*)d_in[1], (const u16*)d_in[2], (const u16*)d_in[3], (const u16*)d_in[4],
                                       (const u16*)d_in[5], (const u16*)d_in[6], (const u16*)d_in[7], (const u16*)d_in[8], (const u16*)d_in[10],
                                       (const u16*)d_in[11], (const u16*)d_in[12], (const u16*)d_in[13], (u16*)d_out, (char*)d_ws);
}
```

```cpp
#include <hip/hip_runtime.h>
#include <stdint.h>

typedef unsigned short u16;
typedef unsigned int u32;
typedef unsigned long long u64;
typedef u32 u32x2 __attribute__((ext_vector_type(2)));
typedef u32 u32x4 __attribute__((ext_vector_type(4)));
typedef float f32x2 __attribute__((ext_vector_type(2)));
typedef float f32x4 __attribute__((ext_vector_type(4)));
typedef float f32x16 __attribute__((ext_vector_type(16)));
typedef short s16x4 __attribute__((ext_vector_type(4)));
typedef __bf16 bf16x2 __attribute__((ext_vector_type(2)));
typedef __bf16 bf16x8 __attribute__((ext_vector_type(8)));
typedef _Float16 f16x2 __attribute__((ext_vector_type(2)));
typedef _Float16 f16x8 __attribute__((ext_vector_type(8)));
#define LDS3 __attribute__((address_space(3)))
#define DEVI __device__ __forceinline__

#define WS_EP   0u
#define WS_S    4096u
#define WS_FA   (64u << 10)
#define WS_FB   (128u << 10)
#define WS_X    (1u << 20)
#define WS_HX   (5u << 20)
#define WS_HH   (7u << 20)
#define WS_DBG  (16u << 20)
#define WS_BYTES (20u << 20)
#define POISON  0xFFFFFFFFu
#define SPIN_CAP 200000
#define NB_W2 16

#define L_WA    0
#define L_WB    65536
#define L_XT    114688
#define L_PR    (L_XT + 8448)
#define L_SM    163328
#define L_ST    (L_SM + 0)
#define L_SC    (L_SM + 32)
#define L_AM    (L_SM + 64)
#define L_FL    (L_SM + 128)
#define L_JK    (L_SM + 256)
#define L_AX    L_Y
#define L_LO    0
#define L_HI    33792
#define L_KC    157200
#define L_R     114688
#define L_U     147968
#define L_Y     157184
#define LDS_BYTES 163840

DEVI float bf_lo(u32 v) { return __uint_as_float(v << 16); }
DEVI float bf_hi(u32 v) { return __uint_as_float(v & 0xFFFF0000u); }
DEVI u32 pk_bf(float a, float b) { f32x2 t = {a, b}; return __builtin_bit_cast(u32, __builtin_convertvector(t, bf16x2)); }
DEVI u32 pk_h(f32x2 t) { return __builtin_bit_cast(u32, __builtin_convertvector(t, f16x2)); }
DEVI float rbf(float a) { return bf_lo(pk_bf(a, a)); }
DEVI f32x2 cmul(f32x2 p, f32x2 c, f32x2 crot) {
  f32x2 t = (f32x2){p.x, p.x} * c;
  return __builtin_elementwise_fma((f32x2){p.y, p.y}, crot, t);
}
DEVI f32x2 rot(f32x2 c) { return (f32x2){-c.y, c.x}; }
DEVI f32x2 cexp(float a, float b) {
  float mag = __builtin_amdgcn_exp2f(a * 1.44269504088896341f);
  const float c_hi = 0.15915494309189535f;
  const float c_lo = (float)(0.15915494309189533577 - (double)c_hi);
  const float n = __builtin_rintf(b * c_hi);
  const float fr = __builtin_fmaf(b, c_lo, __builtin_fmaf(b, c_hi, -n));
  return (f32x2){mag * __builtin_amdgcn_cosf(fr), mag * __builtin_amdgcn_sinf(fr)};
}
template <int CTRL> DEVI float dppf(float v) {
  return __builtin_bit_cast(float, __builtin_amdgcn_update_dpp(0, __builtin_bit_cast(int, v), CTRL, 0xF, 0xF, true));
}
DEVI float wave_sum(float v) {
  v += dppf<0xB1>(v);
  v += dppf<0x4E>(v);
  v += dppf<0x141>(v);
  v += dppf<0x140>(v);
  { auto r = __builtin_amdgcn_permlane16_swap(__float_as_uint(v), __float_as_uint(v), false, false); v = __uint_as_float(r[0]) + __uint_as_float(r[1]); }
  { auto r = __builtin_amdgcn_permlane32_swap(__float_as_uint(v), __float_as_uint(v), false, false); v = __uint_as_float(r[0]) + __uint_as_float(r[1]); }
  return v;
}
DEVI float wave_max(float v) {
  v = fmaxf(v, dppf<0xB1>(v));
  v = fmaxf(v, dppf<0x4E>(v));
  v = fmaxf(v, dppf<0x141>(v));
  v = fmaxf(v, dppf<0x140>(v));
  { auto r = __builtin_amdgcn_permlane16_swap(__float_as_uint(v), __float_as_uint(v), false, false); v = fmaxf(__uint_as_float(r[0]), __uint_as_float(r[1])); }
  { auto r = __builtin_amdgcn_permlane32_swap(__float_as_uint(v), __float_as_uint(v), false, false); v = fmaxf(__uint_as_float(r[0]), __uint_as_float(r[1])); }
  return v;
}
DEVI float rows_sum(float v) {
  { auto r = __builtin_amdgcn_permlane16_swap(__float_as_uint(v), __float_as_uint(v), false, false); v = __uint_as_float(r[0]) + __uint_as_float(r[1]); }
  { auto r = __builtin_amdgcn_permlane32_swap(__float_as_uint(v), __float_as_uint(v), false, false); v = __uint_as_float(r[0]) + __uint_as_float(r[1]); }
  return v;
}
template <int V> struct IC { static constexpr int v = V; constexpr operator int() const { return V; } };
template <int B, int E, typename Fn> DEVI void sfor(Fn&& f) {
  if constexpr (B < E) { f(IC<B>{}); sfor<B + 1, E>(f); }
}
#define BAR() asm volatile("s_waitcnt lgkmcnt(0)\n\ts_barrier" ::: "memory")
#define MEMB() asm volatile("" ::: "memory")
#define VMWAIT0() asm volatile("s_waitcnt vmcnt(0)" ::: "memory")
#define SC1 16
#define W2ST(a, v) lds_st<u32x4>(a, v)
#define W2LD(e) __builtin_amdgcn_raw_buffer_load_b128(r2, voff + ((e) & 1) * 8192, pb2[((e) >> 1) & 3] * 16 + ((e) >> 3) * 16384, 0)
#define SB() __builtin_amdgcn_sched_barrier(0)
#define PIN(x) asm volatile("" : "+v"(x))
#define RAWBAR() asm volatile("s_barrier" ::: "memory")
#define PFW 1
#define DLY1 55
#define NB0 0
#define NB1 0
#define XS_AT 1
#define NB2 54
#define PFX 1
#define NB3 44
#define ROTW1 1
#define W1E 0
#define W1A 0
#define PZ_AT 0
#define TLBW 1
#define T1 0
#define T2 1
#define T3 0
#define T3AT 10
#define T0 0
#define W1P 20
#define PACE 8
#define RSLP 6
#define XSLP 4
#define DLY2 205
#define HGO 95
#define HINT1 1
#define HINT2 1
#define SSLP 0
#define SRT 2
#define B5CAT 1
#define W2L 8
#define LEAD 0
#define PF0 10
#define PF2 13
#define TX1 0
#define TX2 0
#define PKPRIO 3
#define HPRIO 0
#define B6AT 0
#define PRE2 4
#define PACE2 10
#define DENS 4
#define DENS2 2
#define FBD 90
#define FBK 3
#define T3ST 2
#define W2LEAD 1
#define SPRIO 0
#define XD 100
#define T2SLP 2
#define PZ 0
#define CE 1
#define STOUCH 0
#define SALL 0
#define SPRD 1
#define LLAT 8
#define NR 15
#define CTROT 0
#define SDLY 140
#define DENSB DENS
#define TL 1
#define STN 2
#define STP0 4
#define STP1 8
#define STP2 12
#define GPRIO 0
#define FAP 0
#define WSL 0
#define MPRIO 0
#define OUTM 1
#define TPRIO 0
#define OUTAUX 18

DEVI u32x4 mk_rsrc(const void* p, u32 bytes) {
  u64 a = (u64)p;
  u32x4 r = {(u32)a, (u32)(a >> 32) & 0xFFFFu, bytes, 0x00020000u};
  return r;
}
DEVI void dma16(u32 lds, u32 voff, u32x4 rs, u32 soff) {
  asm volatile("s_mov_b32 m0, %0\n\ts_nop 0\n\tbuffer_load_dwordx4 %1, %2, %3 offen lds" ::"s"(lds), "v"(voff), "s"(rs), "s"(soff) : "memory");
}
DEVI void touch(u32 lds, u32 voff, u32x4 rs, u32 soff = 0) {
  asm volatile("s_mov_b32 m0, %0\n\ts_nop 0\n\tbuffer_load_dword %1, %2, %3 offen lds" ::"s"(lds), "v"(voff), "s"(rs), "s"(soff) : "memory");
}
DEVI void set_m0(u32 v) { asm volatile("s_mov_b32 m0, %0\n\ts_nop 0" ::"s"(__builtin_amdgcn_readfirstlane(v)) : "memory"); }
template <int OFF> DEVI void st_tid(u32 v) { asm volatile("ds_write_addtid_b32 %0 offset:%1" ::"v"(v), "n"(OFF) : "memory"); }
template <typename T> DEVI T lds_ld(u32 a) { return *(const LDS3 T*)a; }
template <typename T> DEVI void lds_st(u32 a, T v) { *(LDS3 T*)a = v; }
DEVI s16x4 lds_tr(u32 a) { return __builtin_amdgcn_ds_read_tr16_b64_v4i16((LDS3 s16x4*)a); }
DEVI bf16x8 cat8(s16x4 a, s16x4 b) {
  u32x2 x = __builtin_bit_cast(u32x2, a), y = __builtin_bit_cast(u32x2, b);
  u32x4 r = {x.x, x.y, y.x, y.y};
  return __builtin_bit_cast(bf16x8, r);
}
DEVI f32x4 mfma16(bf16x8 a, bf16x8 b, f32x4 c) { return __builtin_amdgcn_mfma_f32_16x16x32_bf16(a, b, c, 0, 0, 0); }
DEVI f32x16 mfma32(bf16x8 a, bf16x8 b, f32x16 c) { return __builtin_amdgcn_mfma_f32_32x32x16_bf16(a, b, c, 0, 0, 0); }
DEVI void mma16(f32x4& c, bf16x8 a, bf16x8 b) { asm volatile("v_mfma_f32_16x16x32_bf16 %0, %1, %2, %0" : "+v"(c) : "v"(a), "v"(b)); }
DEVI void mma16z(f32x4& c, bf16x8 a, bf16x8 b) { asm volatile("v_mfma_f32_16x16x32_bf16 %0, %1, %2, 0" : "=&v"(c) : "v"(a), "v"(b)); }
DEVI void mma16k16(f32x4& c, s16x4 a, s16x4 b) { asm volatile("v_mfma_f32_16x16x16_bf16 %0, %1, %2, %0" : "+v"(c) : "v"(a), "v"(b)); }
DEVI void mma32(f32x16& c, bf16x8 a, bf16x8 b) { asm volatile("v_mfma_f32_32x32x16_bf16 %0, %1, %2, %0" : "+v"(c) : "v"(a), "v"(b)); }
DEVI void mma32z(f32x16& c, bf16x8 a, bf16x8 b) { asm volatile("v_mfma_f32_32x32x16_bf16 %0, %1, %2, 0" : "=&v"(c) : "v"(a), "v"(b)); }
#define NOPS(n) do { SB(); asm volatile("s_nop " #n); SB(); } while (0)
DEVI u32 max3u(u32 a, u32 b, u32 c) { return max(max(a, b), c); }
DEVI u32 max4(u32 m, u32x4 v) { return max3u(max3u(m, v.x, v.y), v.z, v.w); }

#define STAMP(i)
#define STAMPV(i, val)
#define STAMPC(i)

template <int SET> struct ConvOrd {
  int t[18], q[18];
  constexpr ConvOrd() : t(), q() {
    int n = 0;
    for (int mx = 0; mx < 8; mx++)
      for (int T = 0; T < 8; T++) {
        const bool mine = SET == 0 ? (T == 7 || T == 6 || T == 2) : (T == 5 || T == 4 || T == 3 || T == 1 || T == 0);
        if (!mine) continue;
        for (int qq = 0; qq <= T; qq++) {
          const int m = T - qq;
          if ((qq > m ? qq : m) == mx) { t[n] = T; q[n] = qq; n++; }
        }
      }
  }
};
template <int SET> DEVI void conv_wave(u32 sb, int ch, int lane) {
  constexpr int NF = SET == 0 ? 8 : 6;
  constexpr ConvOrd<SET> O;
  const int b = lane & 15, g = lane >> 4;
  u32 baseA = sb + L_R + ch * 16640 + (b & 3) * 4160 + 2 * (2028 - 4 * (b >> 2) + 8 * g) - 512 * 7 - 128;
  u32 baseB = sb + L_U + ch * 4608 + 2 * (256 + 16 * b - 16 + 8 * g) - 128;
  PIN(baseA); PIN(baseB);
  f32x4 acc[8];
#pragma unroll
  for (int t = 0; t < 8; t++) { acc[t] = (f32x4){0.f, 0.f, 0.f, 0.f}; if (t == O.t[0] || t == O.t[1] || t == O.t[3] || t == O.t[5] || t == O.t[17] || t == O.t[15]) PIN(acc[t]); }
  SB();
  u32x2 Al[2][NF], Ah[2][NF];
  u32x4 B[2][NF];
  auto rd = [&](int buf, int k, int off) {
    const int f = k / 3, j = k % 3;
    if (j == 0) Al[buf][f] = lds_ld<u32x2>(baseA + off + 512 * (7 - f));
    else if (j == 1) Ah[buf][f] = lds_ld<u32x2>(baseA + off + 512 * (7 - f) + 8);
    else B[buf][f] = lds_ld<u32x4>(baseB + off + 512 * f);
  };
#pragma unroll
  for (int k = 0; k < 3 * NF; k++) { rd(0, k, 128); SB(); }
  __builtin_amdgcn_s_waitcnt(SET == 0 ? (0xC07F | (9 << 8)) : 0xC07F);
  SB();
#pragma unroll 1
  for (int it2 = 0; it2 < 4; it2++) {
#pragma unroll
    for (int h = 0; h < 2; h++) {
#pragma unroll
      for (int i = 0; i < 18; i++) {
        const int T = O.t[i], q = O.q[i];
        const u32x4 a = {Al[h][q].x, Al[h][q].y, Ah[h][q].x, Ah[h][q].y};
        mma16(acc[T], __builtin_bit_cast(bf16x8, a), __builtin_bit_cast(bf16x8, B[h][T - q]));
        SB();
        if (SET == 0 && i == 7) { __builtin_amdgcn_s_waitcnt(0xC07F | (11 << 8)); SB(); }
        else if (i == 17) { __builtin_amdgcn_s_waitcnt(SET == 0 ? (0xC07F | (9 << 8)) : 0xC07F); SB(); }
        else {
          const int u = (SET == 0 && i > 7) ? i - 1 : i;
          const int f = u >> 1;
          if (f < NF) {
            if ((u & 1) == 0) { rd(h ^ 1, 3 * f, 64 - 64 * h); SB(); rd(h ^ 1, 3 * f + 1, 64 - 64 * h); SB(); }
            else { rd(h ^ 1, 3 * f + 2, 64 - 64 * h); SB(); }
          }
        }
      }
    }
    baseA -= 128; baseB -= 128;
  }
  NOPS(7);
#pragma unroll
  for (int t = 0; t < 8; t++) {
    const bool mine = SET == 0 ? (t == 7 || t == 6 || t == 2) : (t == 5 || t == 4 || t == 3 || t == 1 || t == 0);
    if (mine) {
      u32x2 v = {pk_bf(acc[t].x, acc[t].y), pk_bf(acc[t].z, acc[t].w)};
      u32 a = (t >= 6 ? sb + L_U + ch * 4608 + 512 : sb + L_Y + ch * 3072) + 512 * t + 32 * b + 8 * g;
      lds_st<u32x2>(a, v);
    }
  }
}

#define L_RB   16384
#define L_PART 34816

struct HOrd {
  int q[56], cnt[29];
  constexpr HOrd() : q(), cnt() {
    int n = 0;
    bool done[56] = {};
    for (int i = 0; i < 56; i++) {
      const int l = i + LEAD;
      if (l < 56 && (l & 7) == 0 && !done[l]) { q[n++] = l; done[l] = true; }
      if (!done[i]) { q[n++] = i; done[i] = true; }
    }
    for (int e = 0; e <= 28; e++) { const int a = PF0 + 3 * e, b = PF2 + 2 * e, m = a < b ? a : b; cnt[e] = m < 56 ? m : 56; }
  }
};

constexpr int EPN = 27;
struct P2Sched {
  int fs[84], fc[84], op[6 * EPN + 14], beg[169];
  constexpr P2Sched() : fs(), fc(), op(), beg() {
    for (int f = 0; f < 84; f++) {
      if (f < 4) { fs[f] = 14 + (f >> 1); fc[f] = f & 1; }
      else { fs[f] = (f - 4) & 15; fc[f] = 2 + ((f - 4) >> 4); }
    }
    int slot[6 * EPN + 14] = {};
    int n = 0;
    for (int k = 0; k < 14; k++) { op[n] = 1000 + k; slot[n] = 4 + k * STN / 4; n++; }
    int pos = 12 * (slot[n - 1] + 2);
    int ax[6] = {};
    for (int c = 0; c < 6; c++) {
      ax[c] = c < 2 ? pos / 12 + c : 8 + 32 * (c - 1);
      for (int k = 0; k < 2; k++) { op[n] = EPN * c + k; slot[n] = ax[c]; n++; }
    }
    for (int c = 0; c < 6; c++) {
      if (pos < 12 * (ax[c] + 5)) pos = 12 * (ax[c] + 5);
      int step = c == 0 ? STP0 : c == 1 ? STP1 : STP2;
      if (pos + step * (EPN - 3) >= 12 * 168) step = (12 * 168 - 1 - pos) / (EPN - 3);
      for (int k = 2; k < EPN; k++) { op[n] = EPN * c + k; slot[n] = pos / 12; pos += step; n++; }
    }
    for (int i = 1; i < n; i++)
      for (int q = i; q > 0 && slot[q - 1] > slot[q]; q--) { int t = slot[q]; slot[q] = slot[q - 1]; slot[q - 1] = t; t = op[q]; op[q] = op[q - 1]; op[q - 1] = t; }
    int q = 0;
    for (int s = 0; s <= 168; s++) { while (q < n && slot[q] < s) q++; beg[s] = q; }
  }
};
static_assert(P2Sched().beg[168] == 6 * EPN + 14, "every filler is scheduled");

DEVI void xsel(u32& o, u32 other, u32 keep) {
  asm volatile("v_cndmask_b32_dpp %0, %1, %2, vcc row_ror:8 row_mask:0xf bank_mask:0xf" : "=&v"(o) : "v"(other), "v"(keep) : "vcc");
}
DEVI void xsel_set(u32& o, u32 other, u32 keep, u64 m) {
  asm volatile("s_mov_b64 vcc, %3\n\tv_cndmask_b32_dpp %0, %1, %2, vcc row_ror:8 row_mask:0xf bank_mask:0xf" : "=&v"(o) : "v"(other), "v"(keep), "s"(m) : "vcc");
}
template <int CTRL> DEVI u32 dppmaxu(u32 v) { return max(v, (u32)__builtin_amdgcn_update_dpp(0u, v, CTRL, 0xF, 0xF, true)); }
DEVI float smul(float s, float v) { float r; asm("v_mul_f32 %0, %1, %2" : "=v"(r) : "s"(s), "v"(v)); return r; }
typedef u64 u64x2 __attribute__((ext_vector_type(2)));
DEVI void strip(u32x4& v, u64 nm) { u64x2 t = __builtin_bit_cast(u64x2, v); t += nm; v = __builtin_bit_cast(u32x4, t); PIN(v); }
DEVI void orin(u32& mx, const u32x4& v) { mx = (mx | v.x | v.y) | v.z | v.w; PIN(mx); }
DEVI void cmpeq(u64& m, u32 v) { m = __builtin_amdgcn_ballot_w64(v == POISON); asm volatile("" : "+s"(m)); }
DEVI void pin2(u32& a, u32x4& b) { asm volatile("" : "+v"(a), "+v"(b)); }
DEVI void pins(u64& v) { asm volatile("" : "+s"(v)); }
DEVI void ldfb(u32x2& v, u32 off, u32x4 rs) { asm volatile("buffer_load_dwordx2 %0, %1, %2, 0 offen sc1" : "=v"(v) : "v"(off), "s"(rs) : "memory"); }
DEVI u32 rclk() { return (u32)__builtin_amdgcn_s_memrealtime(); }
template <int CTRL> DEVI int dppmaxi(int v) { return max(v, __builtin_amdgcn_update_dpp(v, v, CTRL, 0xF, 0xF, false)); }
DEVI int wave_maxi(int v) {
  v = dppmaxi<0x140>(dppmaxi<0x141>(dppmaxi<0x4E>(dppmaxi<0xB1>(v))));
  { auto r = __builtin_amdgcn_permlane16_swap(v, v, false, false); v = max((int)r[0], (int)r[1]); }
  { auto r = __builtin_amdgcn_permlane32_swap(v, v, false, false); v = max((int)r[0], (int)r[1]); }
  return __builtin_amdgcn_readfirstlane(v);
}
DEVI void wait_until(u32 t0, int d) { while ((int)(rclk() - t0) < d) { if (WSL) __builtin_amdgcn_s_sleep(WSL); } }
DEVI u32 flags_wait(u32 a) {
  int sp = 0;
  u32x4 f;
  for (;;) {
    f = *(const volatile LDS3 u32x4*)a;
    if ((__builtin_amdgcn_readfirstlane(f.x & f.y & f.z & f.w) & 1) != 0 || ++sp >= SPIN_CAP * 8) break;
    __builtin_amdgcn_s_sleep(1);
  }
  MEMB();
  return __builtin_amdgcn_readfirstlane(f.x);
}
#define LGKM(n) __builtin_amdgcn_s_waitcnt(0xC07F | ((n) << 8))

__global__ void __launch_bounds__(512, 2)
s4ffn_fused(const u16* __restrict__ x, const u16* __restrict__ Are, const u16* __restrict__ Aim, const u16* __restrict__ Bre,
            const u16* __restrict__ Bim, const u16* __restrict__ Cre, const u16* __restrict__ Cim, const u16* __restrict__ logdt,
            const u16* __restrict__ Dsk, const u16* __restrict__ w1, const u16* __restrict__ b1, const u16* __restrict__ w2,
            const u16* __restrict__ b2, u16* __restrict__ out, char* __restrict__ ws) {
  __shared__ __attribute__((aligned(16))) char smem[LDS_BYTES];
  const u32 sb = (u32)(uintptr_t)(LDS3 char*)smem;
  int tid = threadIdx.x, lane = tid & 63;
  const int w = __builtin_amdgcn_readfirstlane(tid >> 6);
  const int bid = blockIdx.x, xcd = bid & 7, idx = bid >> 3, lin = xcd * 32 + idx;
  STAMP(0);
  STAMPC(28);
  const u16* pa = (const u16*)((const __attribute__((address_space(4))) u64*)__builtin_amdgcn_kernarg_segment_ptr())[w < 6 ? 1 + w : 14];
  asm volatile("" ::"s"(pa), "s"(x), "s"(logdt), "s"(Dsk), "s"(w1), "s"(b1), "s"(w2), "s"(b2), "s"(out), "s"(ws));
  u32x2 sm;
  u32x4 row;
  asm volatile("global_load_dwordx2 %0, %1, %2 sc1" : "=v"(sm) : "v"(lane * 8 + (w < 6 ? lin * 512 : 0)), "s"(pa));
  asm volatile("global_load_dwordx4 %0, %1, %2" : "=v"(row) : "v"(tid * 16 + lin * 8192), "s"(x));
  u32 ldt2, dsk;
  asm volatile("s_load_dword %0, %2, %4\n\ts_load_dword %1, %3, %4" : "=&s"(ldt2), "=&s"(dsk) : "s"(logdt), "s"(Dsk), "s"(lin * 4) : "memory");
  SB();
  const __amdgpu_buffer_rsrc_t rw = __builtin_amdgcn_make_buffer_rsrc(ws, 0, WS_BYTES, 0x00020000);
  const int g1_panel = (xcd >> 1) * 4 + (idx >> 3), g1_cr = (xcd & 1) * 8 + (idx & 7);
  const int g2_tr = xcd * 4 + (idx >> 3), g2_ct = idx & 7;
  const int rot2 = g2_ct + (idx >> 3), rot1 = rot2;
  const int chw = (w >> 1) & 1;
  {
    u64 pc; u32 len;
    asm volatile("s_getpc_b64 %0\nLs4_anchor:\n\ts_mov_b32 %1, Ls4_end-Ls4_anchor" : "=s"(pc), "=s"(len));
    touch(sb + L_JK, tid >= 496 ? (u32)((tid - 496) * 32 + idx) * 128u : 0xFFFFFF00u, mk_rsrc((const void*)pc, len));
  }
  u32 ga[16];
  if (w < 4) {
    const int l0 = 2047 - 8 * tid;
    const float f0 = (float)l0;
#pragma unroll
    for (int i = 0; i < 12; i += 2) {
      const u32 pq = pk_bf(i < 8 ? f0 - i : fmaxf(f0 - i, 0.f), i < 8 ? f0 - (i + 1) : fmaxf(f0 - (i + 1), 0.f));
      ga[i] = sb + L_KC - 4 * 17024 + 4 * max((int)(pq & 0xFFFFu), min(l0 + 17024 - i, 17152));
      if (i < 10) ga[i + 1] = sb + L_KC - 4 * 17024 + 4 * max((int)(pq >> 16), min(l0 + 17023 - i, 17152));
    }
    const int r = lane & 15, g = lane >> 4, p = (w & 1) * 16 + r;
    const int sh = g < 2 ? 5 : 6 - g, ad = g < 2 ? 16 * g : 512 * g;
    ga[11] = sb + L_KC + chw * 2 + (p << sh) + ad;
    ga[12] = ga[11] + (g < 3 ? 8 : 256);
    ga[13] = sb + L_KC + chw * 2 + 4 * (512 + 128 * g + p);
    ga[14] = sb + L_HI + chw * 11088 + min(16 + r, 20) * 528 + g * 16;
    ga[15] = sb + r * 528 + g * 16;
  } else {
#pragma unroll
    for (int p = 0; p < 16; p++) { const u32 jj = p * 64 + lane, q = (jj * 10923u) >> 16; ga[p] = p < 3 ? g1_cr * 224 + q * 3488 + jj * 16 : 0; }
    ga[3] = g1_cr * 224 + 96 + (lane >> 3) * 3584 + ((((lane >> 1) & 3) ^ (lane >> 4)) * 32) + (lane & 1) * 16;
    const u32 l = min(lane, 55);
    const u32 q = l * 128 + (((xcd >> 1) * 32 + idx + l * 37) & 127), qr = (q * 9363u) >> 17;
    ga[7] = qr * 1792u + (xcd & 1) * 1792u + q * 128u;
    ga[8] = (l * 256 + ((lin + l * 37) & 255)) * 128;
    ga[4] = WS_HX + (16 * (w - 4) + (lane >> 3)) * 32768 + lin * 128 + (lane & 7) * 16;
    const u32 za = w == 4 ? L_U + (lane >> 5) * 4608 + (lane & 31) * 16 : (lane & 8) ? L_FL + (lane & 7) * 16 : L_KC - 16;
    if (w < 6) lds_st<u32x4>(sb + za, (u32x4){0, 0, 0, 0});
  }
  u32 xa = sb + L_XT + (2 * (tid & 3)) * 1040 + (tid >> 2) * 4, pra = sb + L_PR + (tid & 255) * 2;
  u32 xo = WS_X + (tid >> 2) * 16384 + (lin * 8 + 2 * (tid & 3)) * 8;
  PIN(xa); PIN(pra); PIN(xo);
#pragma unroll
  for (int i = 0; i < 16; i++) PIN(ga[i]);
  asm volatile("s_waitcnt lgkmcnt(0)" : "+s"(ldt2), "+s"(dsk));
  float dt = rbf(__expf(chw ? bf_hi(ldt2) : bf_lo(ldt2)));
  PIN(dt);
  SB();
  if (PZ == 4 && w >= 4) {
    __builtin_amdgcn_raw_buffer_store_b128((u32x4){POISON, POISON, POISON, POISON}, rw, ga[4], 0, SC1);
    __builtin_amdgcn_raw_buffer_store_b128((u32x4){POISON, POISON, POISON, POISON}, rw, ga[4] + 8 * 32768, 0, SC1);
    asm volatile("s_waitcnt vmcnt(3)" ::: "memory");
  } else {
    asm volatile("s_waitcnt vmcnt(1)" ::: "memory");
  }
  asm volatile("" : "+v"(row), "+v"(sm));
  {
    lds_st<u32x4>(sb + L_XT + w * 1040 + lane * 16, row);

    lds_st<u32x2>(sb + L_PR + w * 512 + lane * 8, sm);
    u32 am = max(max((sm.x << 16) & 0x7FFF0000u, sm.x & 0x7FFF0000u), max((sm.y << 16) & 0x7FFF0000u, sm.y & 0x7FFF0000u));
    am = dppmaxu<0x140>(dppmaxu<0x141>(dppmaxu<0x4E>(dppmaxu<0xB1>(am))));
    { auto r = __builtin_amdgcn_permlane16_swap(am, am, false, false); am = max(r[0], r[1]); }
    lds_st<u32>(sb + L_AM + (lane >> 5) * 32 + w * 4, am);
    const bf16x2 one2 = __builtin_bit_cast(bf16x2, 0x3F803F80u);
    float s = 0.f, q = 0.f;
#pragma unroll
    for (int i = 0; i < 4; i++) {
      const u32 ru = row[i];
      const bf16x2 e = __builtin_bit_cast(bf16x2, ru);
      s = __builtin_amdgcn_fdot2_f32_bf16(e, one2, s, false);
      q = __builtin_amdgcn_fdot2_f32_bf16(e, e, q, false);
    }
    s += dppf<0xB1>(s); q += dppf<0xB1>(q); s += dppf<0x4E>(s); q += dppf<0x4E>(q);
    s += dppf<0x141>(s); q += dppf<0x141>(q); s += dppf<0x140>(s); q += dppf<0x140>(q);
    { auto r = __builtin_amdgcn_permlane16_swap(__float_as_uint(s), __float_as_uint(q), false, false); s = __uint_as_float(r[0]) + __uint_as_float(r[1]); }
    const float m = (__uint_as_float(__builtin_amdgcn_readlane(__float_as_uint(s), 0)) + __uint_as_float(__builtin_amdgcn_readlane(__float_as_uint(s), 32))) * (1.f / 512.f);
    const float q2 = (__uint_as_float(__builtin_amdgcn_readlane(__float_as_uint(s), 16)) + __uint_as_float(__builtin_amdgcn_readlane(__float_as_uint(s), 48))) * (1.f / 512.f);
    const float var = rbf(fmaxf(q2 - m * m, 0.f));
    const float sd = __builtin_amdgcn_sqrtf(rbf(var + 1e-5f));
    lds_st<u32>(sb + L_ST + w * 4, pk_bf(m, sd));
  }
  STAMP(1);
  BAR();
  u32 tag, are, aim, bre, bim, cre, cim;
  f32x2 amb, amc;
  {
    const int tq = tid & 3;
    const u32 ep = lds_ld<u32>(sb + L_PR + 6 * 512);
    const u32x2 s2 = lds_ld<u32x2>(sb + L_ST + tq * 8);
    const u32 a0 = lds_ld<u32>(xa), a1 = lds_ld<u32>(xa + 1040), a2 = lds_ld<u32>(xa + 512), a3 = lds_ld<u32>(xa + 1552);
    are = lds_ld<u16>(pra); aim = lds_ld<u16>(pra + 512);
    bre = lds_ld<u16>(pra + 1024); bim = lds_ld<u16>(pra + 1536); cre = lds_ld<u16>(pra + 2048); cim = lds_ld<u16>(pra + 2560);
    amb = lds_ld<f32x2>(sb + L_AM + chw * 32 + 8); amc = lds_ld<f32x2>(sb + L_AM + chw * 32 + 16);
    SB();
    tag = __builtin_amdgcn_readfirstlane(ep) * 1664525u + 1013904223u;
    if (w == 0) __builtin_amdgcn_raw_buffer_store_b128((u32x4){s2.x, tag, s2.y, tag}, rw, tid < 4 ? WS_S + (lin * 8 + 2 * tid) * 8 : 0xFFFFFF00u, 0, SC1);
    __builtin_amdgcn_raw_buffer_store_b128((u32x4){a0, tag, a1, tag}, rw, xo, 0, SC1);
    __builtin_amdgcn_raw_buffer_store_b128((u32x4){a2, tag, a3, tag}, rw, xo + 128 * 16384, 0, SC1);
  }
  STAMP(2);
  float zr = dt * bf_lo(are), zi = dt * bf_lo(aim);

  if (w < 4) {
    if (MPRIO) __builtin_amdgcn_s_setprio(MPRIO);
    {
      const f32x2 c = cexp(zr * 8.f, -zi * 8.f), cr = rot(c);
      const f32x2 c2 = cmul(c, c, cr);
      const f32x2 c4 = cmul(c2, c2, rot(c2)), c4r = rot(c4);
      f32x2 v[4] = {{1.f, 0.f}, c, c2, cmul(c2, c, cr)};
      set_m0(sb + L_LO + chw * 16896 + (w & 1) * 256);
      sfor<0, 8>([&](auto p_) {
        constexpr int p = decltype(p_)::v * 4;
        f32x2 t[4];
        u32 h[4];
#pragma unroll
        for (int i = 0; i < 4; i++) { if (p + 4 < 32) t[i] = (f32x2){v[i].x, v[i].x} * c4; h[i] = pk_h(v[i]); }
        SB();
        sfor<0, 4>([&](auto i_) {
          constexpr int i = decltype(i_)::v;
          if constexpr (p + 4 < 32) v[i] = __builtin_elementwise_fma((f32x2){v[i].y, v[i].y}, c4r, t[i]);
          st_tid<(p + i) * 528>(h[i]);
          SB();
        });
      });
    }
    STAMP(3);
    BAR();
    {
      u32 aH0 = ga[15] + L_HI + chw * 11088, aH1 = ga[14], aL = ga[15] + L_LO + chw * 16896 + (w & 1) * 8448;
      PIN(aH0); PIN(aL);
      f16x8 h0[8], h1[8], l[8];
#pragma unroll
      for (int s = 0; s < 8; s++) { l[s] = lds_ld<f16x8>(aL + s * 64); h0[s] = lds_ld<f16x8>(aH0 + s * 64); h1[s] = lds_ld<f16x8>(aH1 + s * 64); }
      const float sc = lds_ld<float>(sb + L_SC + chw * 4);
      if (w == 0) lds_st<u32x4>(sb + L_R + ((lane >> 2) & 7) * 4160 + 4096 + (lane & 3) * 16, (u32x4){0, 0, 0, 0});
      SB();
      f32x4 k0 = {0.f, 0.f, 0.f, 0.f}, k1 = {0.f, 0.f, 0.f, 0.f};
#pragma unroll
      for (int s = 0; s < 8; s++) {
        k0 = __builtin_amdgcn_mfma_f32_16x16x32_f16(h0[s], l[s], k0, 0, 0, 0);
        k1 = __builtin_amdgcn_mfma_f32_16x16x32_f16(h1[s], l[s], k1, 0, 0, 0);
      }
      const u32 d0 = pk_bf(k0.x * sc, k0.y * sc), d1 = pk_bf(k0.z * sc, k0.w * sc), e0 = pk_bf(k1.x * sc, k1.y * sc), e1 = pk_bf(k1.z * sc, k1.w * sc);
      lds_st<u16>(ga[11], (u16)d0); lds_st<u16>(ga[11] + 4, (u16)(d0 >> 16)); lds_st<u16>(ga[12], (u16)d1); lds_st<u16>(ga[12] + 4, (u16)(d1 >> 16));
      lds_st<u16>(ga[13], (u16)e0); lds_st<u16>(ga[13] + 128, (u16)(e0 >> 16)); lds_st<u16>(ga[13] + 256, (u16)e1); lds_st<u16>(ga[13] + 384, (u16)(e1 >> 16));
    }
    STAMP(4);
    MEMB();
    *(volatile LDS3 u32*)(sb + L_FL + 48 + w * 4) = 1;
    {
      u32 wv[11];
      for (int sp = 0;; sp++) {
        const u32x4 f = *(const volatile LDS3 u32x4*)(sb + L_FL + 48);
#pragma unroll
        for (int i = 0; i < 11; i++) wv[i] = *(const volatile LDS3 u32*)ga[i];
        if (__builtin_amdgcn_readfirstlane(f.x & f.y & f.z & f.w) != 0 || sp >= SPIN_CAP) break;
      }
      u32 a = sb + L_R + tid * 16;
      PIN(a);
#pragma unroll
      for (int ch = 0; ch < 2; ch++)
#pragma unroll
        for (int c = 3; c >= 0; c--) {
          u32x4 d;
#pragma unroll
          for (int i = 0; i < 4; i++) { u32 t; asm("v_perm_b32 %0, %1, %2, %3" : "=v"(t) : "v"(wv[4 - c + 2 * i]), "v"(wv[3 - c + 2 * i]), "s"(ch ? 0x07060302u : 0x05040100u), "n"(c)); d[i] = t; }
          lds_st<u32x4>(a + ch * 16640 + c * 4160, d);
        }
    }
    STAMP(5);
    BAR();
    STAMP(6);
    if (w & 1) conv_wave<1>(sb, w >> 1, lane); else conv_wave<0>(sb, w >> 1, lane);
    STAMP(8);
    if (CE) *(volatile LDS3 u32*)(sb + L_SC + 8) = rclk();
    BAR();
    const int r = lane & 15, g = lane >> 4;
    const int tb1 = g1_panel * 4 + w;
    const u32 hh_out = WS_HH + (tb1 * 112 + g1_cr * 7) * 1024 + lane * 16;
    u32 wbg[5], hso[8];
#pragma unroll
    for (int c = 0; c < 4; c++) wbg[c] = sb + L_WA + (lane >> 2) * 128 + ((c ^ (lane >> 3)) & 3) * 32 + (lane & 3) * 8;
    wbg[4] = sb + L_WB + (lane >> 2) * 96 + (lane & 3) * 8;
#pragma unroll
    for (int c = 0; c < 5; c++) PIN(wbg[c]);
#pragma unroll
    for (int q = 0; q < 8; q++) hso[q] = ((q + 2 * rot1) & 7) * 4096;
    auto wrd = [&](int s, int c, int e) { return c >= 3 ? lds_tr(wbg[c - 3] + (s * 32 + 16 * e) * 128) : lds_tr(wbg[4] + (s * 32 + 16 * e) * 96 + c * 32); };
    const u32 hx_in = WS_HX + tb1 * 32768 + lane * 16;
    u32x4 raw[32];
    auto ldraw = [&](int i) { raw[i] = __builtin_amdgcn_raw_buffer_load_b128(rw, hx_in + (i & 3) * 1024, hso[i >> 2], SC1); };
    RAWBAR();
    STAMP(10);
#pragma unroll
    for (int i = 0; i < 12; i++) ldraw(i);
    SB();
    STAMP(9);
    const u32x4 o4 = {0x3F803F80u, 0x3F803F80u, 0x3F803F80u, 0x3F803F80u};
    bf16x8 ones = __builtin_bit_cast(bf16x8, o4);
    PIN(ones);
#pragma unroll
    for (int i = 12; i < 16; i++) ldraw(i);
    SB();
    u32 F[16][2][4];
    f32x4 acc[7][2];
    f32x4 sacc[2], gacc[2];
    u64 mlo = 0x00FF00FF00FF00FFull, mhi = 0xFF00FF00FF00FF00ull;
    asm volatile("" : "+s"(mlo), "+s"(mhi));
    auto frag = [&](int s, int t) { return __builtin_bit_cast(bf16x8, (u32x4){F[s][t][0], F[s][t][1], F[s][t][2], F[s][t][3]}); };
    int retries = 0;
    u32 tp1 = 0;
    auto retry1 = [&](auto gi_, auto hi_, u64 bad) {
      constexpr int gi = decltype(gi_)::v, hi = decltype(hi_)::v;
      if (__builtin_expect(bad != 0, 0)) {
        int sp = 0;
        u32 mx;
        do {
          __builtin_amdgcn_s_sleep(RSLP);
#pragma unroll
          for (int i = 4 * gi; i < hi; i++) ldraw(i);
          mx = max4(max4(max4(max4(0u, raw[4 * gi]), raw[4 * gi + 1]), raw[4 * gi + 2]), raw[4 * gi + 3]);
          pin2(mx, raw[4 * gi]); pin2(mx, raw[4 * gi + 1]); pin2(mx, raw[4 * gi + 2]); pin2(mx, raw[4 * gi + 3]);
          __builtin_amdgcn_s_waitcnt(0x0F70);
          MEMB();
          retries++;
        } while (__any(mx == POISON) && ++sp < SPIN_CAP);
      }
    };
    s16x4 wf[2][2][2], wb[84][2];
    constexpr P2Sched PS;
#pragma unroll
    for (int c = 0; c < 2; c++) { wf[0][c][0] = wrd(0, c, 0); wf[0][c][1] = wrd(0, c, 1); }
    u64 sgm = ((tag ^ lane) & 1) ? 0x8000800080008000ull : 0ull;
    u32 axa = sb + L_AX + lane * 8, shf = g == 3 ? 16 : 0;
    u32x2 tokaux[2];
    tokaux[0].y = 0; tokaux[1].y = 0;
    u64 m1 = __builtin_amdgcn_ballot_w64(r & 1), m2 = __builtin_amdgcn_ballot_w64(r & 2), md = __builtin_amdgcn_ballot_w64(g == (r >> 2)), mo = __builtin_amdgcn_ballot_w64(g & 1);
    u32 fla = sb + L_FL + 16 + w * 4;
    PIN(fla);
    PIN(sgm); PIN(axa); PIN(shf); PIN(tokaux[0]); PIN(tokaux[1]);
    asm volatile("" : "+s"(m1), "+s"(m2), "+s"(md), "+s"(mo));
    SB();
    retry1(IC<0>{}, IC<16>{}, __builtin_amdgcn_ballot_w64(max4(max4(max4(max4(0u, raw[0]), raw[1]), raw[2]), raw[3]) == POISON));
    STAMP(18);
    float rs[2], sa, sb_, sc_;
    s16x4 aux[7];
    u32x4 afl;
    u64 nax;
    auto sel = [](float a, float b, u64 m) { float o; asm("v_cndmask_b32 %0, %1, %2, %3" : "=v"(o) : "v"(a), "v"(b), "s"(m)); return o; };
    auto stat = [&](auto k_) {
      constexpr int k = decltype(k_)::v;
      if constexpr (k == 0) { PIN(gacc[0]); sa = sel(gacc[0].x, gacc[0].y, m1); sb_ = sel(gacc[0].z, gacc[0].w, m1); }
      if constexpr (k == 1) { sa = sel(0.f, sel(sa, sb_, m2), md); }
      if constexpr (k == 2) { PIN(gacc[1]); sc_ = sel(gacc[1].x, gacc[1].y, m1); sb_ = sel(gacc[1].z, gacc[1].w, m1); }
      if constexpr (k == 3) { sb_ = sel(0.f, sel(sc_, sb_, m2), md); }
      if constexpr (k == 4) { PIN(sacc[0]); PIN(sacc[1]); sc_ = sel(sacc[0].x, sacc[1].x, mo) * (-1.f / 512.f); }
      if constexpr (k == 5) { auto q = __builtin_amdgcn_permlane16_swap(__float_as_uint(sa), __float_as_uint(sb_), false, false); sa = __uint_as_float(q[0]) + __uint_as_float(q[1]); sb_ = sa; }
      if constexpr (k == 6) { auto q = __builtin_amdgcn_permlane32_swap(__float_as_uint(sa), __float_as_uint(sb_), false, false); sa = __uint_as_float(q[0]) + __uint_as_float(q[1]); sb_ = sc_ * sc_; }
      if constexpr (k == 7) { sa = __int_as_float(max(__float_as_int(__builtin_fmaf(sa, 1.f / 512.f, -sb_)), 0)); }
      if constexpr (k == 8) { sa = rbf(sa) + 1e-5f; }
      if constexpr (k == 9) { sa = __builtin_amdgcn_sqrtf(rbf(sa)); }
      if constexpr (k == 10) { sb_ = __uint_as_float(pk_bf(sc_, sa)); sa = __builtin_amdgcn_rcpf(bf_hi(__float_as_uint(sb_))); sc_ = sb_; }
      if constexpr (k == 11) { auto q = __builtin_amdgcn_permlane16_swap(__float_as_uint(sb_), __float_as_uint(sc_), false, false); tokaux[0].x = q[0] >> shf; tokaux[1].x = q[1] >> shf; PIN(tokaux[0]); PIN(tokaux[1]); }
      if constexpr (k == 12) { sa = sa * 0x1p-40f; sb_ = sa; }
      if constexpr (k == 13) { auto q = __builtin_amdgcn_permlane16_swap(__float_as_uint(sa), __float_as_uint(sb_), false, false); rs[0] = __uint_as_float(q[0]); rs[1] = __uint_as_float(q[1]); PIN(rs[0]); PIN(rs[1]); }
      if constexpr (k < 13) { PIN(sa); PIN(sb_); if constexpr (k >= 2) PIN(sc_); }
    };
    auto wbrd = [&](int n) { wb[n >> 1][n & 1] = wrd(PS.fs[n >> 1], PS.fc[n >> 1], n & 1); };
    auto statmm = [&](auto s_, auto q_) {
      constexpr int s = decltype(s_)::v, q = decltype(q_)::v, t = q >> 1;
      if constexpr (!(q & 1)) { if constexpr (s == 0) mma16z(gacc[t], frag(s, t), frag(s, t)); else mma16(gacc[t], frag(s, t), frag(s, t)); }
      else { if constexpr (s == 0) mma16z(sacc[t], ones, frag(s, t)); else mma16(sacc[t], ones, frag(s, t)); }
      SB();
    };
    sfor<0, 8>([&](auto b_) {
      constexpr int b = decltype(b_)::v;
      u32 mx = 0;
      u64 bad = 0;
      sfor<0, 16>([&](auto i_) {
        constexpr int i = decltype(i_)::v, k = i & 7, hs = i >> 3, s = 2 * b + hs;
        if constexpr (!(k & 1)) {
          if constexpr (s >= 2) {
            constexpr int sm = s - 2, c = k >> 2, t = (k >> 1) & 1;
            if constexpr (sm == 0) mma16z(acc[c][t], cat8(wf[0][c][0], wf[0][c][1]), frag(sm, t));
            else mma16(acc[c][t], cat8(wf[sm & 1][c][0], wf[sm & 1][c][1]), frag(sm, t));
            SB();
          }
        } else if constexpr (s >= 1) statmm(IC<s - 1>{}, IC<(k >> 1)>{});
        if constexpr (s >= 2 && k < 4) {
          if constexpr (s < 15) wf[(s - 1) & 1][k >> 1][k & 1] = wrd(s - 1, k >> 1, k & 1);
          else wbrd(k);
          SB();
        }
        {
          constexpr int t = k & 1, l = (k >> 1) & 1, hi = (k >> 2) ^ hs;
          if constexpr (i == 0 || k == 4) {
            if constexpr (hi) xsel_set(F[s][0][1], raw[2 * s][0], raw[2 * s][2], mhi); else xsel_set(F[s][0][0], raw[2 * s][2], raw[2 * s][0], mlo);
          } else if constexpr (hi) xsel(F[s][t][2 * l + 1], raw[2 * s + l][t], raw[2 * s + l][2 + t]);
          else xsel(F[s][t][2 * l], raw[2 * s + l][2 + t], raw[2 * s + l][t]);
        }
        constexpr int li = 6 * b + 16 + 3 * hs;
        if constexpr (k >= 5 && li + k - 5 < 32) ldraw(li + k - 5);
        if constexpr (b < 7 && (i == 5 || i == 6 || i == 12 || i == 13)) {
          constexpr int q = 4 * b + 4 + (i < 8 ? i - 5 : i - 10);
          mx = max3u(mx, raw[q].x, raw[q].z); pin2(mx, raw[q]);
        }
        if constexpr (b < 7 && i == 13) cmpeq(bad, mx);
        if constexpr (b == 5 && i == 4) { afl = *(const volatile LDS3 u32x4*)(sb + L_FL); tp1 = rclk(); }
        if constexpr (b == 5 && i == 14) { afl.x &= afl.y & afl.z; afl.x &= afl.w; afl.x -= 1; PIN(afl.x); }
        if constexpr (b == 6 && i == 7) cmpeq(nax, afl.x);
        if constexpr (b == 7 && hs == 0 && k >= 4) { aux[2 * (k - 4)] = lds_ld<s16x4>(axa + 2 * (k - 4) * 512); if constexpr (k < 7) aux[2 * (k - 4) + 1] = lds_ld<s16x4>(axa + (2 * (k - 4) + 1) * 512); }
        if constexpr (b == 7 && hs == 1 && k >= 4) wbrd(k);
        if constexpr (k == 7 && s >= 2) { if constexpr (b == 7) LGKM(4); else LGKM(0); }
        SB();
      });
      if constexpr (b == 6) { if (__builtin_expect(nax != 0, 0)) flags_wait(sb + L_FL); MEMB(); }
      if constexpr (b < 7) retry1(IC<b + 1>{}, IC<(6 * b + 22 < 32 ? 6 * b + 22 : 32)>{}, bad);
    });
    NOPS(0);
    sfor<0, 4>([&](auto q_) { statmm(IC<15>{}, q_); });
    STAMP(11);
    STAMPV(30, retries);
    float ev[4];
    u32x2 pk[2];
    auto epi = [&](auto c_, auto k_) {
      constexpr int c = decltype(c_)::v, k = decltype(k_)::v;
      if constexpr (k < 2) mma16k16(acc[c][k], aux[c], __builtin_bit_cast(s16x4, tokaux[k]));
      else if constexpr (k < 22) {
        constexpr int t = (k - 2) / 10, j = (k - 2) % 10;
        if constexpr (j < 4) asm volatile("v_mul_f32 %0, %1, %2 clamp" : "=v"(ev[j]) : "v"(rs[t]), "v"(acc[c][t][j]));
        else if constexpr (j < 8) asm volatile("v_mul_f32 %0, %0, %0" : "+v"(ev[j - 4]));
        else { u32 o; asm volatile("v_cvt_pk_bf16_f32 %0, %1, %2" : "=v"(o) : "v"(ev[2 * (j - 8)]), "v"(ev[2 * (j - 8) + 1])); pk[t][j - 8] = o; }
      } else if constexpr (k < 24) {
        asm volatile("v_lshl_add_u64 %0, %0, 0, %1" : "+v"(pk[k - 22]) : "v"(sgm));
      } else if constexpr (k < 26) {
        u32 a = pk[0][k - 24], b = pk[1][k - 24];
        if constexpr (k == 24) asm volatile("s_nop 1\n\tv_permlane16_swap_b32 %0, %1" : "+v"(a), "+v"(b));
        else asm volatile("v_permlane16_swap_b32 %0, %1" : "+v"(a), "+v"(b));
        pk[0][k - 24] = a; pk[1][k - 24] = b;
      } else {
        __builtin_amdgcn_raw_buffer_store_b128((u32x4){pk[0].x, pk[0].y, pk[1].x, pk[1].y}, rw, hh_out + c * 1024, 0, SC1);
      }
    };
    sfor<0, 168>([&](auto n_) {
      constexpr int n = decltype(n_)::v, c = PS.fc[n >> 1], s = PS.fs[n >> 1], t = n & 1;
      if constexpr (s == 0) mma16z(acc[c][t], cat8(wb[n >> 1][0], wb[n >> 1][1]), frag(s, t));
      else mma16(acc[c][t], cat8(wb[n >> 1][0], wb[n >> 1][1]), frag(s, t));
      SB();
      if constexpr (n + 8 < 168) { wbrd(n + 8); SB(); }
      if constexpr (n == 40) { *(volatile LDS3 u32*)fla = tp1 | 1; SB(); }
      sfor<PS.beg[n], PS.beg[n + 1]>([&](auto q_) {
        constexpr int o = PS.op[decltype(q_)::v];
        if constexpr (o >= 1000) stat(IC<o - 1000>{}); else epi(IC<o / EPN>{}, IC<o % EPN>{});
        SB();
      });
      if constexpr ((n & 3) == 3 && n + 8 < 168) { LGKM(4); SB(); }
    });
    STAMP(12);
    epi(IC<6>{}, IC<0>{}); epi(IC<6>{}, IC<1>{});
    *(volatile LDS3 u32*)(fla + 48) = 1;
    NOPS(7);
    sfor<2, EPN>([&](auto k_) { epi(IC<6>{}, k_); });
    SB();
    STAMP(13);
    {
      const __amdgpu_buffer_rsrc_t rb2 = __builtin_amdgcn_make_buffer_rsrc((void*)b2, 0, 512 * 2, 0x00020000);
      const u32 b2v = __builtin_amdgcn_raw_buffer_load_b32(rb2, g2_ct * 128 + (w * 8 + (lane >> 3)) * 4, 0, 0);
      u32x4 rv[2];
      const u32 ra = WS_HX + (g2_tr * 2) * 32768 + (g2_ct * 32 + w * 8) * 128 + lane * 16;
      int sp = 0;
      for (;;) {
        rv[0] = __builtin_amdgcn_raw_buffer_load_b128(rw, ra, 0, SC1);
        rv[1] = __builtin_amdgcn_raw_buffer_load_b128(rw, ra + 32768, 0, SC1);
        if (__all(max4(max4(0u, rv[0]), rv[1]) != POISON) || ++sp >= SPIN_CAP) break;
        MEMB();
      }
      flags_wait(sb + L_FL + 64);
      const u32 a = sb + L_RB + (4 * (lane & 7)) * 272 + (w * 8 + (lane >> 3)) * 8;
#pragma unroll
      for (int i = 0; i < 2; i++)
#pragma unroll
        for (int k = 0; k < 4; k++) lds_st<f32x2>(a + (32 * i + k) * 272, (f32x2){bf_lo(rv[i][k]) + bf_lo(b2v), bf_hi(rv[i][k]) + bf_hi(b2v)});
    }
    BAR();
    RAWBAR();
  STAMP(15);
  STAMPC(29);
    asm volatile("s_endpgm" : "=v"(tid), "=v"(lane), "=v"(zr), "=v"(zi), "=v"(dt), "=v"(bre), "=v"(bim), "=v"(cre), "=v"(cim), "=v"(amb), "=v"(amc),
                 "=v"(ga[0]), "=v"(ga[1]), "=v"(ga[2]), "=v"(ga[3]), "=v"(ga[4]), "=v"(ga[5]), "=v"(ga[6]), "=v"(ga[7]), "=v"(ga[8])::"memory");
  }
  {
    const int j = w - 4;
    __builtin_amdgcn_s_setprio(TPRIO);
    const u32 hxo = ga[4];
    asm volatile("" ::"v"(zr), "v"(bre), "v"(amc));
    const u32 tpub = rclk();
    if (PZ == 0) {
    __builtin_amdgcn_raw_buffer_store_b128((u32x4){POISON, POISON, POISON, POISON}, rw, hxo, 0, SC1);
    __builtin_amdgcn_raw_buffer_store_b128((u32x4){POISON, POISON, POISON, POISON}, rw, hxo + 8 * 32768, 0, SC1);
    SB();
    }
    {
      const f32x2 e1 = cexp(zr, zi), e256 = cexp(zr * 256.f, zi * 256.f);
      const int ex = __builtin_amdgcn_frexp_expf((amb.x + amb.y) * (amc.x + amc.y)) - 10;
      const f32x2 cc = {__builtin_ldexpf(bf_lo(cre), -ex), __builtin_ldexpf(bf_lo(cim), -ex)};
      const f32x2 e1r = rot(e1), e256r = rot(e256);
      const f32x2 W = cmul((f32x2){bf_lo(bre), bf_lo(bim)}, cc, rot(cc)), e2 = cmul(e1, e1, e1r), e512 = cmul(e256, e256, e256r);
      const f32x2 e2r = rot(e2), e512r = rot(e512);
      lds_st<float>(sb + L_SC + chw * 4, __builtin_ldexpf(dt, ex));
      set_m0(sb + L_HI + chw * 11088 + (w & 1) * 256);
      const f32x2 e4 = cmul(e2, e2, e2r), v1 = cmul(W, e1, e1r), G1 = cmul(W, e256, e256r), G2 = cmul(W, e512, e512r), v2 = cmul(W, e2, e2r);
      const f32x2 e4r = rot(e4);
      SB();
      const f32x2 v3 = cmul(v1, e2, e2r), G3 = cmul(G1, e512, e512r), G4 = cmul(G2, e512, e512r), t1 = cmul(G1, e2, e2r);
      st_tid<0>(pk_h(W)); st_tid<528>(pk_h(v1)); st_tid<2 * 528>(pk_h(v2)); st_tid<8 * 528>(pk_h(G1)); st_tid<12 * 528>(pk_h(G2));
      SB();
      const f32x2 G5 = cmul(G3, e512, e512r), G6 = cmul(G4, e512, e512r), v4 = cmul(W, e4, e4r), v5 = cmul(v1, e4, e4r);
      st_tid<3 * 528>(pk_h(v3)); st_tid<14 * 528>(pk_h(G3)); st_tid<16 * 528>(pk_h(G4)); st_tid<9 * 528>(pk_h(t1));
      SB();
      const f32x2 G7 = cmul(G5, e512, e512r), G8 = cmul(G6, e512, e512r), v6 = cmul(v2, e4, e4r), v7 = cmul(v3, e4, e4r);
      st_tid<17 * 528>(pk_h(G5)); st_tid<18 * 528>(pk_h(G6)); st_tid<4 * 528>(pk_h(v4)); st_tid<5 * 528>(pk_h(v5));
      SB();
      const f32x2 t2 = cmul(G1, e4, e4r), t3 = cmul(t1, e4, e4r), u2 = cmul(G2, e4, e4r), u3 = cmul(G3, e4, e4r);
      st_tid<19 * 528>(pk_h(G7)); st_tid<20 * 528>(pk_h(G8)); st_tid<6 * 528>(pk_h(v6)); st_tid<7 * 528>(pk_h(v7));
      SB();
      st_tid<10 * 528>(pk_h(t2)); st_tid<11 * 528>(pk_h(t3)); st_tid<13 * 528>(pk_h(u2)); st_tid<15 * 528>(pk_h(u3));
    }
    STAMP(3);
    __builtin_amdgcn_s_setprio(0);
    BAR();
    const u32x4 r1 = mk_rsrc(w1, 512 * 1792 * 2);
    auto w1dma = [&](int i) {
      const int blk = i / 7, k = i % 7, ka = k < 6 ? k >> 1 : 3;
      const u32 so = (((blk + rot1) & 3) * 128 + j * 32) * 3584;
      if (k == 6 || !(k & 1)) dma16(sb + L_WA + (blk * 16 + j * 4 + ka) * 1024, ga[3], r1, so + ka * 8 * 3584);
      else dma16(sb + L_WB + (blk * 12 + j * 3 + ka) * 1024, ga[ka], r1, so);
    };
    const u32x4 r2s = mk_rsrc(w2, 1792 * 512 * 2), rws = mk_rsrc(ws, WS_BYTES);
    const u32 tag2 = ~tag * 0x9E3779B1u;
    u32 xv[8], uv[8];
    {
      u32x4 gx[4], gs[4];
      const u32 o = (512 * j + 2 * lane) * 8;
      int sp = 0;
      auto bad4 = [&](const u32x4* gq) {
        u32 a = 0;
#pragma unroll
        for (int i = 0; i < 4; i++) a |= __builtin_amdgcn_bitop3_b32(gq[i].y, gq[i].w, tag, 0x7E);
        return a;
      };
      if (SSLP) wait_until(tpub, SSLP);
#pragma unroll
      for (int i = 0; i < 4; i++) gs[i] = __builtin_amdgcn_raw_buffer_load_b128(rw, WS_S + o + i * 1024, 0, SC1);
      if (PZ == 1) {
    __builtin_amdgcn_raw_buffer_store_b128((u32x4){POISON, POISON, POISON, POISON}, rw, hxo, 0, SC1);
    __builtin_amdgcn_raw_buffer_store_b128((u32x4){POISON, POISON, POISON, POISON}, rw, hxo + 8 * 32768, 0, SC1);
    SB();
      }
      STAMP(4);
      if (__builtin_expect(__any(bad4(gs) != 0), 0)) {
        do {
          __builtin_amdgcn_s_sleep(SRT);
          if (bad4(gs) != 0) {
#pragma unroll
            for (int i = 0; i < 4; i++) gs[i] = __builtin_amdgcn_raw_buffer_load_b128(rw, WS_S + o + i * 1024, 0, SC1);
          }
          __builtin_amdgcn_s_waitcnt(0x0F70);
          MEMB();
        } while (__any(bad4(gs) != 0) && ++sp < SPIN_CAP);
        __builtin_amdgcn_s_sleep(XSLP);
        sp += 1;
      }
      wait_until(tpub, XD);
      STAMP(22);
#pragma unroll
      for (int i = 0; i < 4; i++) asm volatile("buffer_load_dwordx4 %0, %1, %2, 0 offen offset:%3 sc1" : "=v"(gx[i]) : "v"(WS_X + lin * 16384 + o), "s"(rws), "n"(i * 1024) : "memory");
      u32 lastw = 0;
      if (TL) {
        if (lane == 0) lastw = __hip_atomic_fetch_add((LDS3 u32*)(sb + L_FL + 80), 1u, __ATOMIC_RELAXED, __HIP_MEMORY_SCOPE_WORKGROUP);
        lastw = __builtin_amdgcn_readfirstlane(lastw) == 3 ? 0u : 0xFF000000u;
        touch(sb + L_JK, ga[7] | lastw, r1);
        if (TL == 2) touch(sb + L_JK, ga[8] | lastw, r2s);
      }
      asm volatile("s_waitcnt vmcnt(%4)" : "+v"(gx[0]), "+v"(gx[1]), "+v"(gx[2]), "+v"(gx[3]) : "n"(TL) : "memory");
      if (__builtin_expect(__any(bad4(gx) != 0), 0)) {
        do {
          __builtin_amdgcn_s_sleep(SRT);
#pragma unroll
          for (int i = 0; i < 4; i++) gx[i] = __builtin_amdgcn_raw_buffer_load_b128(rw, WS_X + lin * 16384 + o + i * 1024, 0, SC1);
          __builtin_amdgcn_s_waitcnt(0x0F70);
          MEMB();
          sp += 100;
        } while (__any(bad4(gx) != 0) && sp < SPIN_CAP * 100);
      }
      STAMPV(30, sp);
      STAMP(5);
#pragma unroll
      for (int i = 0; i < 8; i++) {
        const u32 xx = gx[i >> 1][(i & 1) * 2], st = gs[i >> 1][(i & 1) * 2];
        const float m = bf_lo(st), ri = __builtin_amdgcn_rcpf(bf_hi(st));
        const u32 d = pk_bf(bf_lo(xx) - m, bf_hi(xx) - m);
        xv[i] = xx;
        uv[i] = pk_bf(bf_lo(d) * ri, bf_hi(d) * ri);
      }
      u32 a = sb + L_U + 512 + (512 * j + 2 * lane) * 2;
      PIN(a);
#pragma unroll
      for (int i = 0; i < 4; i++) {
        lds_st<u32>(a + i * 256, __builtin_amdgcn_perm(uv[2 * i + 1], uv[2 * i], 0x05040100u));
        lds_st<u32>(a + i * 256 + 4608, __builtin_amdgcn_perm(uv[2 * i + 1], uv[2 * i], 0x07060302u));
      }
    }
    if (PZ == 2) VMWAIT0();
    BAR();
    STAMP(6);
    __builtin_amdgcn_s_setprio(HPRIO);
    const u32 tb3 = rclk();
    if (j == 0) __builtin_amdgcn_raw_buffer_store_b128((u32x4){tag, tag2, tb3, 0}, rw, lane == 0 ? WS_FA + lin * 16 : 0xFFFFFF00u, 0, SC1);
    if (T2 == 1 && j == 3) touch(sb + L_JK, ga[8], r2s);
#pragma unroll
    for (int i = 0; i < 10; i++) w1dma(i);
    SB();
    u32 voff[2];
    voff[0] = (j * 448 + (lane >> 3)) * 1024 + g2_ct * 128 + (((lane & 7) ^ (((lane >> 4) & 1) << 2)) * 16);
    voff[1] = voff[0] + 8192;
    u32 pb[4];
#pragma unroll
    for (int p = 0; p < 4; p++) pb[p] = ((p + rot2) & 3) * 7168;
    constexpr int ND = NR + 5;
    auto w2dma = [&](int e) {
      const u32 d = sb + (e < ND ? L_R + (e % 5) * 8192 : 49152 + ((e - ND + 2) & 7) * 8192) + j * 2048;
      dma16(d, voff[0], r2s, pb[e & 3] * 16 + (e >> 2) * 16384);
      dma16(d + 1024, voff[1], r2s, pb[e & 3] * 16 + (e >> 2) * 16384);
    };
    const int q4 = (lane >> 2) & 3;
    const u32 tro = sb + j * 2048 + ((lane >> 5) * 8 + q4) * 128 + ((lane >> 4) & 1) * 32 + (lane & 3) * 8;
    u32 trb[4];
#pragma unroll
    for (int i = 0; i < 4; i++) trb[i] = tro + (((q4 >> 1) ^ j ^ i) & 1) * 64 + (i < 2 ? L_R : 49152);
    PIN(trb[0]); PIN(trb[1]);
    auto w2rd = [&](int e, int blk) { const u32 a = trb[(e < ND ? 0 : 2) + blk] + (e < ND ? e % 5 : (e - ND + 2) & 7) * 8192; return cat8(lds_tr(a), lds_tr(a + 512)); };
    bf16x8 w2f[28][2];
#pragma unroll
    for (int i = 0; i < 8; i++) { uv[i] = pk_bf(smul(bf_lo(dsk), bf_lo(uv[i])), smul(bf_hi(dsk), bf_hi(uv[i]))); PIN(uv[i]); }
    f32x2 uf[8], xf[8];
#pragma unroll
    for (int i = 0; i < 8; i++) { uf[i] = (f32x2){bf_lo(uv[i]), bf_hi(uv[i])}; xf[i] = (f32x2){bf_lo(xv[i]), bf_hi(xv[i])}; PIN(uf[i]); PIN(xf[i]); }
    u32 hs4[4], fao = WS_FA + (tid - 256) * 16;
#pragma unroll
    for (int i = 0; i < 4; i++) { hs4[i] = WS_HX + (16 * j + 4 * i + (lane >> 4)) * 32768 + lin * 128 + (lane & 15) * 8; PIN(hs4[i]); }
    u32 ya = (j == 3 ? sb + L_U + 512 : sb + L_Y) + (512 * j + 2 * lane) * 2, yc = ya + (j == 3 ? 4608 : 3072);
    PIN(voff[0]); PIN(voff[1]); PIN(fao); PIN(ya); PIN(yc);
    SB();
    STAMP(26);
#pragma unroll
    for (int i = 10; i < W1P; i++) { w1dma(i); if (T2 == i && j == 3) touch(sb + L_JK, ga[8], r2s); }
    const int ct0 = j < 2 ? j : 2 * j - 1;
    u32 b1v[2];
#pragma unroll
    for (int i = 0; i < 2; i++) asm volatile("buffer_load_ushort %0, %1, %2, 0 offen" : "=v"(b1v[i]) : "v"((g1_cr * 112 + (ct0 + i) * 16 + (lane & 15)) * 2), "s"(mk_rsrc(b1, 1792 * 2)) : "memory");
    if (bid == 0 && tid == 256) __builtin_amdgcn_raw_buffer_store_b32(tag, rw, WS_EP, 0, SC1);
    STAMP(8);
    RAWBAR();
    STAMP(9);
    u32x4 fa;
    {
      u32 y0[4], y1[4];
#pragma unroll
      for (int i = 0; i < 4; i++) { y0[i] = lds_ld<u32>(ya + i * 256); y1[i] = lds_ld<u32>(yc + i * 256); }
      SB();
      if (FAP == 0) asm volatile("buffer_load_dwordx4 %0, %1, %2, 0 offen sc1" : "=v"(fa) : "v"(fao), "s"(rws) : "memory");
#pragma unroll
      for (int i = 0; i < 4; i++) {
        u32 hv[2];
#pragma unroll
        for (int k = 0; k < 2; k++) {
          const f32x2 y = {k ? bf_hi(y0[i]) : bf_lo(y0[i]), k ? bf_hi(y1[i]) : bf_lo(y1[i])};
          const f32x2 t = y + uf[2 * i + k];
          const u32 s = pk_bf(t.x, t.y);
          const f32x2 h = xf[2 * i + k] + (f32x2){bf_lo(s), bf_hi(s)};
          hv[k] = pk_bf(h.x, h.y);
        }
        __builtin_amdgcn_raw_buffer_store_b64((u32x2){hv[0], hv[1]}, rw, hs4[i], 0, SC1);
        SB();
      }
      if (FAP == 1) asm volatile("buffer_load_dwordx4 %0, %1, %2, 0 offen sc1" : "=v"(fa) : "v"(fao), "s"(rws) : "memory");
    }
    STAMP(10);
    MEMB();
#pragma unroll
    for (int i = W1P; i < 28 - W1A; i++) { w1dma(i); }
    if (T2 == 2) { __builtin_amdgcn_s_sleep(T2SLP); touch(sb + L_JK, j == 3 ? ga[8] : 0xFFFFFF00u, r2s); }
#pragma unroll
    for (int e = 0; e < PRE2; e++) w2dma(e);
    asm volatile("s_waitcnt vmcnt(%3)" : "+v"(fa), "+v"(b1v[0]), "+v"(b1v[1]) : "n"((FAP ? 28 : 32) - W1A - W1P + 2 * PRE2 + (T2 == 2)) : "memory");
    if (__builtin_expect(__any(fa.x != tag || fa.y != tag2), 0)) {
      int sp = 0;
      do { __builtin_amdgcn_s_sleep(16); fa = __builtin_amdgcn_raw_buffer_load_b128(rw, fao, 0, SC1); __builtin_amdgcn_s_waitcnt(0x0F70); MEMB(); } while (__any(fa.x != tag || fa.y != tag2) && ++sp < SPIN_CAP);
    }
    {
      const int late = HINT1 ? min(max(wave_maxi((int)(fa.z - tb3)), 0), 400) : 0;
      STAMPV(21, late);
      wait_until(__builtin_amdgcn_readfirstlane(*(const volatile LDS3 u32*)(sb + L_SC + 8)), late + DLY1);
    }
    asm volatile("s_waitcnt vmcnt(%0)" ::"n"(2 * PRE2 + (T2 == 2) + (W1P == 28 ? 4 : 0)) : "memory");
    STAMP(27);
    RAWBAR();
    const u32 tb5a = rclk();
    {
      const u32x4 o4 = {0x3F803F80u, 0x3F803F80u, 0x3F803F80u, 0x3F803F80u};
      bf16x8 ones = __builtin_bit_cast(bf16x8, o4);
      f32x4 cs[2];
      auto colsum = [&](auto str_, u32 a0, u32 a1) {
        constexpr int str = decltype(str_)::v;
        PIN(a0); PIN(a1); PIN(ones);
        SB();
        s16x4 wq[16][4];
        auto rdq = [&](int s) {
#pragma unroll
          for (int i = 0; i < 4; i++) wq[s][i] = lds_tr(((i >> 1) ? a1 : a0) + (32 * s + 16 * (i & 1)) * str);
        };
#pragma unroll
        for (int s = 0; s < 3; s++) rdq(s);
        SB();
        sfor<0, 16>([&](auto s_) {
          constexpr int s = decltype(s_)::v;
          if constexpr (s == 0) { mma16z(cs[0], ones, cat8(wq[s][0], wq[s][1])); mma16z(cs[1], ones, cat8(wq[s][2], wq[s][3])); }
          else { mma16(cs[0], ones, cat8(wq[s][0], wq[s][1])); mma16(cs[1], ones, cat8(wq[s][2], wq[s][3])); }
          SB();
          if constexpr (s + 3 < 16) { rdq(s + 3); SB(); }
        });
        NOPS(7);
        PIN(cs[0]); PIN(cs[1]);
      };
      if (j >= 2) {
        const u32 a = sb + L_WA + (lane >> 2) * 128 + (lane & 3) * 8;
        colsum(IC<128>{}, a + (((ct0 - 3) ^ (lane >> 3)) & 3) * 32, a + (((ct0 - 2) ^ (lane >> 3)) & 3) * 32);
      } else {
        const u32 a = sb + L_WB + (lane >> 2) * 96 + (lane & 3) * 8 + ct0 * 32;
        colsum(IC<96>{}, a, a + 32);
      }
#pragma unroll
      for (int i = 0; i < 2; i++) {
        const float v = cs[i].x, h0 = rbf(v), h1 = rbf(v - h0), h2 = v - h0 - h1;
        const int g = lane >> 4;
        const u32 val = g == 3 ? b1v[i] : pk_bf(g == 0 ? h0 : g == 1 ? h1 : h2, 0.f);
        lds_st<u32x2>(sb + L_AX + (ct0 + i) * 512 + lane * 8, (u32x2){val, 0});
      }
    }
    STAMP(20);
    asm volatile("s_waitcnt lgkmcnt(0)" ::: "memory");
    *(volatile LDS3 u32*)(sb + L_FL + j * 4) = 1;
    if (j == 0) __builtin_amdgcn_raw_buffer_store_b64((u32x2){tag, tb5a}, rw, lane == 0 ? WS_FB + (g1_panel * 16 + g1_cr) * 8 : 0xFFFFFF00u, 0, SC1);
    STAMP(11);
    const u32 fbo = WS_FB + ((g2_tr >> 1) * 16 + j * 4 + (lane & 3)) * 8;
    u32x2 fb;
    if (SDLY) wait_until(tb5a, SDLY);
    sfor<PRE2, ND>([&](auto e_) {
      constexpr int e = decltype(e_)::v;
      if constexpr (e >= 5) {
        asm volatile("s_waitcnt vmcnt(8)" ::: "memory");
        w2f[e - 5][0] = w2rd(e - 5, 0); w2f[e - 5][1] = w2rd(e - 5, 1);
        PIN(w2f[e - 5][0]); PIN(w2f[e - 5][1]);
      }
      if constexpr (e == ND - 6) ldfb(fb, fbo, rws);
      w2dma(e);
    });
    asm volatile("" : "+v"(fb));
    if (__builtin_expect(__any(fb.x != tag), 0)) {
      int sp = 0;
      do { __builtin_amdgcn_s_sleep(8); fb = __builtin_amdgcn_raw_buffer_load_b64(rw, fbo, 0, SC1); __builtin_amdgcn_s_waitcnt(0x0F70); MEMB(); } while (__any(fb.x != tag) && ++sp < 64);
    }
    const int late2 = min(max(__builtin_amdgcn_readfirstlane(dppmaxi<0x4E>(dppmaxi<0xB1>((int)(fb.y - tb5a)))), 0), 300);
    STAMP(12);
    u64 smask = ((tag ^ lane) & 1) ? 0ull - 0x8000800080008000ull : 0ull;
    u32 sbits = 0x80008000u;
    PIN(smask);
    asm volatile("" : "+s"(sbits));
    const int tbo = (j >> 1) ^ (g2_ct >> 2);
    u32 hin[4];
#pragma unroll
    for (int i = 0; i < 4; i++) { hin[i] = WS_HH + ((g2_tr * 2 + (tbo ^ (i & 1))) * 112 + j * 28) * 1024 + lane * 16 + (i >> 1) * 4096; PIN(hin[i]); }
    u32x4 hf[56];
    auto ldh = [&](int q) { hf[q] = __builtin_amdgcn_raw_buffer_load_b128(rw, hin[(q & 1) + ((q >> 5) << 1)] + ((q >> 3) & 3) * 1024, pb[(q >> 1) & 3], SC1); };
    __builtin_amdgcn_s_setprio(GPRIO);
    const u32 tfl = flags_wait(sb + L_FL + 16);
    STAMP(7);
#pragma unroll
    for (int e = ND; e < ND + 6; e++) { asm volatile("s_waitcnt vmcnt(%0)" ::"n"(PACE2) : "memory"); w2dma(e); }
    STAMP(16);
    wait_until(tfl, late2 + DLY2);
    STAMP(13);
    constexpr HOrd HO;
#pragma unroll
    for (int q = 0; q < HO.cnt[0]; q++) ldh(HO.q[q]);
    SB();
    __builtin_amdgcn_s_setprio(0);
    f32x16 oc[2][2];
    int retries2 = 0;
    auto sgc = [&](u32 m, u32x4& v) { strip(v, smask); orin(m, v); return m; };
    auto retry2 = [&](auto e0_, auto n_, auto hi_, u64 bad) {
      constexpr int e0 = decltype(e0_)::v, n = decltype(n_)::v, hi = decltype(hi_)::v;
      if (__builtin_expect(bad != 0, 0)) {
        u32 mx;
        int sp = 0;
        do {
          __builtin_amdgcn_s_sleep(RSLP);
#pragma unroll
          for (int e = 0; e < hi; e++) if (HO.q[e] >= e0) ldh(HO.q[e]);
          mx = 0;
#pragma unroll
          for (int e = e0; e < e0 + n; e++) mx = sgc(mx, hf[e]);
          __builtin_amdgcn_s_waitcnt(0x0F70);
          MEMB();
          retries2++;
        } while (__any((mx & sbits) != 0) && ++sp < SPIN_CAP);
      }
    };
    retry2(IC<0>{}, IC<4>{}, IC<HO.cnt[0]>{}, __builtin_amdgcn_ballot_w64((sgc(sgc(sgc(sgc(0u, hf[0]), hf[1]), hf[2]), hf[3]) & sbits) != 0));
    STAMP(19);
    auto mm2 = [&](auto e_, auto a_, auto b_) {
      constexpr int e = decltype(e_)::v, a = decltype(a_)::v, b = decltype(b_)::v;
      const bf16x8 wv = w2f[e][b], hb = __builtin_bit_cast(bf16x8, hf[2 * e + a]);
      if constexpr (e == 0) mma32z(oc[a][b], wv, hb); else mma32(oc[a][b], wv, hb);
      SB();
    };
    f32x4 rb[4];
    u32 rba, xs[4], oa;
    u32x4 p2f;
    u64 np2;
    sfor<0, 12>([&](auto gi_) {
      constexpr int gi = decltype(gi_)::v, q1 = 4 * gi + 4;
      u32 mx = 0;
      u64 bad = 0;
      sfor<0, 8>([&](auto s_) {
        constexpr int s = decltype(s_)::v, e = 2 * gi + (s >> 2), m = s & 3, i0 = HO.cnt[e], i1 = HO.cnt[e + 1];
        if constexpr (m == 0) {
          if constexpr (e == 23) RAWBAR();
          if constexpr (e == LLAT && ND + 6 < 28) { if (__builtin_expect(np2 != 0, 0)) flags_wait(sb + L_FL + 64); MEMB(); }
          if constexpr (e == 8) STAMP(23);
          if constexpr (e == 16) STAMP(24);
        }
        mm2(IC<e>{}, IC<(m >> 1)>{}, IC<(m & 1)>{});
        if constexpr (e == ND - 5 && m == 0) { PIN(trb[2]); PIN(trb[3]); }
        if constexpr (e + 4 >= NR && (s == 0 || s == 6)) w2f[e + 4][0] = w2rd(e + 4, 0);
        if constexpr (e + 4 >= NR && (s == 2 || s == 7)) w2f[e + 4][1] = w2rd(e + 4, 1);
        if constexpr (e == LLAT - 2 && m == 2) p2f = *(const volatile LDS3 u32x4*)(sb + L_FL + 64);
        if constexpr (e == LLAT - 1 && m == 2) { u32 t = (p2f.x & p2f.y & p2f.z & p2f.w) - 1; cmpeq(np2, t); }
        if constexpr (e >= LLAT && ND + 6 + e - LLAT < 28 && m == 2) w2dma(ND + 6 + e - LLAT);
        if constexpr (m == 1 && i0 < i1) ldh(HO.q[i0]);
        if constexpr (m == 2 && i0 + 2 < i1) ldh(HO.q[i0 + 1]);
        if constexpr (m == 3 && i0 + 1 < i1) ldh(HO.q[i1 - 1]);
        SB();
        if constexpr (s == 0) strip(hf[q1], smask);
        if constexpr (s == 1) { strip(hf[q1 + 1], smask); orin(mx, hf[q1]); }
        if constexpr (s == 2) orin(mx, hf[q1 + 1]);
        if constexpr (s == 3) strip(hf[q1 + 2], smask);
        if constexpr (s == 4) { strip(hf[q1 + 3], smask); orin(mx, hf[q1 + 2]); }
        if constexpr (s == 5) orin(mx, hf[q1 + 3]);
        if constexpr (gi == 11) {
          if constexpr (s == 3) strip(hf[52], smask);
          if constexpr (s == 5) { strip(hf[53], smask); orin(mx, hf[52]); }
          if constexpr (s == 6) { strip(hf[54], smask); orin(mx, hf[53]); }
          if constexpr (s == 7) { strip(hf[55], smask); orin(mx, hf[54]); orin(mx, hf[55]); }
        }
        if constexpr (s == (gi == 11 ? 7 : 5)) { bad = __builtin_amdgcn_ballot_w64((mx & sbits) != 0); pins(bad); }
        if constexpr (s == 6 && gi == 4) {
          const int sl = lane & 31, tok = tbo * 32 + 4 * (sl & 7) + 2 * ((sl >> 3) & 1) + (sl >> 4);
          rba = tok; PIN(rba);
        }
        if constexpr (s == 7 && gi == 4) {
          oa = (g2_tr * 64 + rba) * 1024 + g2_ct * 128 + (j & 1) * 64 + (lane >> 5) * 16;
          rba = sb + L_RB + rba * 272 + (j & 1) * 128 + (lane >> 5) * 16;
          PIN(rba); PIN(oa);
        }
        if constexpr (s >= 6 && (gi == 2 || gi == 3)) { constexpr int x = 2 * (gi - 2) + s - 6; xs[x] = sb + ((j ^ x) == 3 ? L_PART : L_R + (j ^ x) * 12288) + lane * 16; PIN(xs[x]); }
        SB();
      });
      retry2(IC<q1>{}, IC<(gi == 11 ? 8 : 4)>{}, IC<HO.cnt[2 * gi + 2]>{}, bad);
    });
    STAMP(25);
    {
      auto send = [&](auto i_, auto k_) {
        constexpr int i = decltype(i_)::v, k = decltype(k_)::v;
        const f32x16& o = oc[i >> 1][i & 1];
        lds_st<f32x4>(xs[i] + (i - 1) * 4096 + k * 1024, (f32x4){o[4 * k], o[4 * k + 1], o[4 * k + 2], o[4 * k + 3]});
      };
      sfor<0, 16>([&](auto n_) {
        constexpr int n = decltype(n_)::v, i = 3 - n / 4, e = 24 + n % 4;
        mm2(IC<e>{}, IC<(i >> 1)>{}, IC<(i & 1)>{});
        if constexpr (n < 4) rb[n] = lds_ld<f32x4>(rba + n * 32);
        if constexpr (n >= 5 && (n & 3) == 1) { NOPS(1); send(IC<i + 1>{}, IC<0>{}); send(IC<i + 1>{}, IC<1>{}); }
        if constexpr (n >= 5 && (n & 3) >= 2) send(IC<i + 1>{}, IC<(n & 3)>{});
        SB();
      });
      NOPS(11);
      STAMP(14);
      STAMPV(31, retries2);
#pragma unroll
      for (int i = 0; i < 4; i++) { rb[i] += 0x1p80f * (f32x4){oc[0][0][4 * i], oc[0][0][4 * i + 1], oc[0][0][4 * i + 2], oc[0][0][4 * i + 3]}; PIN(rb[i]); }
      BAR();
      u32 ob[4][2];
      f32x4 pp[3][4];
#pragma unroll
      for (int i = 0; i < 4; i++)
#pragma unroll
        for (int q = 0; q < 3; q++) pp[q][i] = lds_ld<f32x4>(xs[0] + q * 4096 + i * 1024);
      SB();
#pragma unroll
      for (int i = 0; i < 4; i++) {
        const f32x4 v = rb[i] + 0x1p80f * ((pp[0][i] + pp[1][i]) + pp[2][i]);
        ob[i][0] = pk_bf(v.x, v.y); ob[i][1] = pk_bf(v.z, v.w);
      }
      const __amdgpu_buffer_rsrc_t ro = __builtin_amdgcn_make_buffer_rsrc(out, 0, 2048 * 512 * 2, 0x00020000);
      {
        const int sl = lane & 31, tk = 4 * (sl & 7) + 2 * ((sl >> 3) & 1) + (sl >> 4);
        const u32 ta = sb + j * 4096;
#pragma unroll
        for (int i = 0; i < 4; i++) lds_st<u32x2>(ta + tk * 80 + i * 16 + (lane >> 5) * 8, (u32x2){ob[i][0], ob[i][1]});
        MEMB();
        const u32x4 q0 = lds_ld<u32x4>(ta + (lane >> 2) * 80 + (lane & 3) * 16), q1 = lds_ld<u32x4>(ta + (16 + (lane >> 2)) * 80 + (lane & 3) * 16);
        const u32 ob0 = (g2_tr * 64 + tbo * 32 + (lane >> 2)) * 1024 + g2_ct * 128 + (j & 1) * 64 + (lane & 3) * 16;
        __builtin_amdgcn_raw_buffer_store_b128(q0, ro, ob0, 0, OUTAUX);
        __builtin_amdgcn_raw_buffer_store_b128(q1, ro, ob0 + 16384, 0, OUTAUX);
      }
    }
  }
  asm volatile("Ls4_end:");
  STAMP(15);
  STAMPC(29);
}

extern "C" void kernel_launch(void* const* d_in, const int* in_sizes, int n_in, void* d_out, int out_size, void* d_ws, size_t ws_size,
                              hipStream_t stream) {
  s4ffn_fused<<<256, 512, 0, stream>>>((const u16*)d_in[0], (const u16*)d_in[1], (const u16*)d_in[2], (const u16*)d_in[3], (const u16*)d_in[4],
                                       (const u16*)d_in[5], (const u16*)d_in[6], (const u16*)d_in[7], (const u16*)d_in[8], (const u16*)d_in[10],
                                       (const u16*)d_in[11], (const u16*)d_in[12], (const u16*)d_in[13], (u16*)d_out, (char*)d_ws);
}
```
